```python
import torch

def _softmax(x, axis=-1):
    return torch.softmax(x, dim=axis)
def _gelu(x):
    return torch.nn.functional.gelu(x, approximate="tanh")
def _silu(x):
    return torch.nn.functional.silu(x)
def _sigmoid(x):
    return torch.sigmoid(x)
def _layernorm(x, axis=-1, eps=1e-5):
    m = x.mean(dim=axis, keepdim=True)
    v = x.var(dim=axis, keepdim=True, unbiased=False)
    return (x - m) / torch.sqrt(v + eps)
def _scatter_add(tgt, idx, src, axis=0):
    return tgt.clone().index_add_(axis, idx, src)
def _segment_sum(src, seg, n):
    out = torch.zeros((n,) + src.shape[1:], dtype=src.dtype)
    return out.index_add_(0, seg, src)
def _pad(x, pw):
    flat = tuple(v for pair in reversed(pw) for v in pair)
    return torch.nn.functional.pad(x, flat)

kernel_name = 'parallel_sum__linear_kernel_hedgehog__mixer_token_mlp__prompt'

def setup_inputs():

    _w = (lambda a: (lambda t: t.to(torch.bfloat16) if t.is_floating_point() else t)(torch.as_tensor(a)))
    def _A_setup():
        B, S, D, H = 1, 2048, 512, 32
        Dh = D // H
        return {
            "x": _w((lambda *s: torch.randn(*s, dtype=torch.bfloat16) * 0.05)(B, S, D)),
            "wqkv": _w((lambda *s: torch.randn(*s, dtype=torch.bfloat16) * 0.2)(D, 3 * D)),
            "w_phi": _w((lambda *s: torch.randn(*s, dtype=torch.bfloat16) * 0.2)(H, Dh, Dh) * (Dh ** -0.5)),
            "wo": _w((lambda *s: torch.randn(*s, dtype=torch.bfloat16) * 0.05)(D, D)),
        }
    _A_in = _A_setup()

    def _B_setup():
        B, S, D, F = 1, 2048, 512, 1376
        return {
            "x": _w((lambda *s: torch.randn(*s, dtype=torch.bfloat16) * 0.05)(B, S, D)),
            "w1": _w((lambda *s: torch.randn(*s, dtype=torch.bfloat16) * 0.8)(S, F) * (S ** -0.5)),
            "b1": _w((lambda *s: torch.randn(*s, dtype=torch.bfloat16) * 3.2)(F) * 0.02),
            "w2": _w((lambda *s: torch.randn(*s, dtype=torch.bfloat16) * 0.4)(F, S) * (F ** -0.5)),
            "b2": _w((lambda *s: torch.randn(*s, dtype=torch.bfloat16) * 1.6)(S) * 0.02),
        }
    _B_in = _B_setup()

    return {
        "A_x": _A_in["x"],
        "A_wqkv": _A_in["wqkv"],
        "A_w_phi": _A_in["w_phi"],
        "A_wo": _A_in["wo"],
        "B_x": _B_in["x"],
        "B_w1": _B_in["w1"],
        "B_b1": _B_in["b1"],
        "B_w2": _B_in["w2"],
        "B_b2": _B_in["b2"],
    }

def reference(A_x, A_wqkv, A_w_phi, A_wo, B_x, B_w1, B_b1, B_w2, B_b2):
    def _A_ref(x, wqkv, w_phi, wo):
        B, S, D = x.shape
        H = 32; Dh = D // H
        qkv = (lambda x, *d: x.permute(*d))((x @ wqkv).reshape(B, S, 3, H, Dh), 2, 0, 3, 1, 4)
        q, k, v = qkv[0], qkv[1], qkv[2]
        pq = torch.einsum("bhsd,hdf->bhsf", q, w_phi)
        pk = torch.einsum("bhsd,hdf->bhsf", k, w_phi)
        phi_q = torch.concatenate([_softmax(pq, axis=-1), _softmax(-pq, axis=-1)], axis=-1)
        phi_k = torch.concatenate([_softmax(pk, axis=-1), _softmax(-pk, axis=-1)], axis=-1)
        kv = torch.einsum("bhsf,bhsd->bhfd", phi_k, v)
        z  = torch.einsum("bhsf->bhf", phi_k)
        num = torch.einsum("bhqf,bhfd->bhqd", phi_q, kv)
        den = torch.einsum("bhqf,bhf->bhq", phi_q, z)[..., None] + 1e-6
        o = num / den
        return (lambda x, *d: x.permute(*d))(o, 0, 2, 1, 3).reshape(B, S, D) @ wo

    def _B_ref(x, w1, b1, w2, b2):
        B, S, D = x.shape
        h = _layernorm(x, axis=-1)
        u = torch.einsum("bsd,sf->bdf", h, w1) + b1
        u = _gelu(u)
        y = torch.einsum("bdf,fs->bds", u, w2) + b2
        return (lambda x, *d: x.permute(*d))(y, 0, 2, 1)

    x = A_x
    xn = _layernorm(x, axis=-1)
    return x + _A_ref(xn, A_wqkv, A_w_phi, A_wo) + _B_ref(xn, B_w1, B_b1, B_w2, B_b2)
```

```cpp
#include <hip/hip_runtime.h>
#include <hip/hip_bf16.h>
typedef __hip_bfloat16 bf16;
constexpr int S=2048, D=512, H=32, DH=16, F=1376;
__device__ __forceinline__ float b2f(bf16 x){return __bfloat162float(x);}
__device__ __forceinline__ bf16 f2b(float x){return __float2bfloat16(x);}
__device__ __forceinline__ float rb(float x){return b2f(f2b(x));}

__global__ void k_ln(const bf16* x, bf16* xn, bf16* h){
  int row=blockIdx.x; int t=threadIdx.x;
  float v[8]; float s=0;
  for(int i=0;i<8;i++){v[i]=b2f(x[row*D+t*8+i]); s+=v[i];}
  for(int o=32;o>0;o>>=1) s+=__shfl_xor(s,o);
  float m=s/D; float q=0;
  for(int i=0;i<8;i++){float d=v[i]-m; q+=d*d;}
  for(int o=32;o>0;o>>=1) q+=__shfl_xor(q,o);
  float r=1.0f/sqrtf(q/D+1e-5f);
  s=0;
  for(int i=0;i<8;i++){v[i]=rb((v[i]-m)*r); xn[row*D+t*8+i]=f2b(v[i]); s+=v[i];}
  for(int o=32;o>0;o>>=1) s+=__shfl_xor(s,o);
  m=s/D; q=0;
  for(int i=0;i<8;i++){float d=v[i]-m; q+=d*d;}
  for(int o=32;o>0;o>>=1) q+=__shfl_xor(q,o);
  r=1.0f/sqrtf(q/D+1e-5f);
  for(int i=0;i<8;i++){h[row*D+t*8+i]=f2b((v[i]-m)*r);}
}
__global__ void k_qkv(const bf16* xn, const bf16* w, bf16* qkv){
  int n=blockIdx.x*256+threadIdx.x; int m=blockIdx.y;
  float acc=0;
  for(int k=0;k<D;k++) acc+=b2f(xn[m*D+k])*b2f(w[k*3*D+n]);
  qkv[m*3*D+n]=f2b(acc);
}
__global__ void k_phi(const bf16* qkv, const bf16* wphi, bf16* phiq, bf16* phik){
  int idx=blockIdx.x*256+threadIdx.x; int which=idx&1; int h=(idx>>1)&31; int s=idx>>6;
  float q[16]; for(int d=0;d<16;d++) q[d]=b2f(qkv[s*3*D+which*D+h*16+d]);
  float p[16]; float mx=-1e30f, mn=1e30f;
  for(int f=0;f<16;f++){float a=0; for(int d=0;d<16;d++) a+=q[d]*b2f(wphi[h*256+d*16+f]); p[f]=rb(a); mx=fmaxf(mx,p[f]); mn=fminf(mn,p[f]);}
  float s1=0,s2=0;
  for(int f=0;f<16;f++){s1+=__expf(p[f]-mx); s2+=__expf(mn-p[f]);}
  bf16* o=(which?phik:phiq)+(size_t)(s*H+h)*32;
  for(int f=0;f<16;f++){o[f]=f2b(__expf(p[f]-mx)/s1); o[16+f]=f2b(__expf(mn-p[f])/s2);}
}
__global__ void k_kv(const bf16* phik, const bf16* qkv, float* kv, float* z){
  int h=blockIdx.x; int t=threadIdx.x;
  int f=t>>4, d=t&15; float a=0, zz=0;
  for(int s=0;s<S;s++){float p=b2f(phik[(size_t)(s*H+h)*32+f]); a+=p*b2f(qkv[s*3*D+2*D+h*16+d]); zz+=p;}
  kv[h*512+t]=rb(a); if(d==0) z[h*32+f]=rb(zz);
}
__global__ void k_o(const bf16* phiq, const float* kv, const float* z, bf16* o){
  int idx=blockIdx.x*256+threadIdx.x; int d=idx&15; int h=(idx>>4)&31; int s=idx>>9;
  float num=0, den=0;
  for(int f=0;f<32;f++){float p=b2f(phiq[(size_t)(s*H+h)*32+f]); num+=p*kv[h*512+f*16+d]; den+=p*z[h*32+f];}
  o[s*D+h*16+d]=f2b(num/(den+1e-6f));
}
__global__ void k_u(const bf16* h, const bf16* w1, const bf16* b1, bf16* u){
  int f=blockIdx.x*256+threadIdx.x; int d=blockIdx.y; if(f>=F) return;
  float a=0; for(int s=0;s<S;s++) a+=b2f(h[s*D+d])*b2f(w1[s*F+f]);
  a+=b2f(b1[f]);
  float g=0.5f*a*(1.0f+tanhf(0.7978845608028654f*(a+0.044715f*a*a*a)));
  u[d*F+f]=f2b(g);
}
__global__ void k_out(const bf16* x, const bf16* o, const bf16* wo, const bf16* u, const bf16* w2, const bf16* b2, bf16* out){
  int s=blockIdx.x*256+threadIdx.x; int d=blockIdx.y;
  float a=0; for(int j=0;j<D;j++) a+=b2f(o[s*D+j])*b2f(wo[j*D+d]);
  float b=0; for(int f=0;f<F;f++) b+=b2f(u[d*F+f])*b2f(w2[f*S+s]);
  b+=b2f(b2[s]);
  out[s*D+d]=f2b(b2f(x[s*D+d])+a+b);
}
extern "C" void kernel_launch(void* const* d_in, const int* in_sizes, int n_in, void* d_out, int out_size, void* d_ws, size_t ws_size, hipStream_t stream){
  const bf16* x=(const bf16*)d_in[0]; const bf16* wqkv=(const bf16*)d_in[1]; const bf16* wphi=(const bf16*)d_in[2]; const bf16* wo=(const bf16*)d_in[3];
  const bf16* w1=(const bf16*)d_in[5]; const bf16* b1=(const bf16*)d_in[6]; const bf16* w2=(const bf16*)d_in[7]; const bf16* b2=(const bf16*)d_in[8];
  char* ws=(char*)d_ws; size_t MB=1<<20;
  bf16* xn=(bf16*)(ws); bf16* h=(bf16*)(ws+2*MB); bf16* qkv=(bf16*)(ws+4*MB); bf16* phiq=(bf16*)(ws+10*MB); bf16* phik=(bf16*)(ws+14*MB);
  float* kv=(float*)(ws+18*MB); float* z=(float*)(ws+19*MB); bf16* o=(bf16*)(ws+20*MB); bf16* u=(bf16*)(ws+22*MB);
  k_ln<<<S,64,0,stream>>>(x,xn,h);
  k_qkv<<<dim3(6,S),256,0,stream>>>(xn,wqkv,qkv);
  k_phi<<<S*64/256,256,0,stream>>>(qkv,wphi,phiq,phik);
  k_kv<<<H,512,0,stream>>>(phik,qkv,kv,z);
  k_o<<<S*D/256,256,0,stream>>>(phiq,kv,z,o);
  k_u<<<dim3(6,D),256,0,stream>>>(h,w1,b1,u);
  k_out<<<dim3(8,D),256,0,stream>>>(x,o,wo,u,w2,b2,(bf16*)d_out);
}
```

```cpp
#include <hip/hip_runtime.h>
#include <hip/hip_bf16.h>

namespace hk {
#define DI __device__ __forceinline__
#define LAS __attribute__((address_space(3)))
typedef short bf16x8 __attribute__((ext_vector_type(8)));
typedef short s16x4 __attribute__((ext_vector_type(4)));
typedef float f32x16 __attribute__((ext_vector_type(16)));
typedef float f32x4 __attribute__((ext_vector_type(4)));
typedef float f32x2 __attribute__((ext_vector_type(2)));
typedef unsigned u32x4 __attribute__((ext_vector_type(4)));
typedef unsigned u32x2 __attribute__((ext_vector_type(2)));
typedef __bf16 bfx2 __attribute__((ext_vector_type(2)));
typedef __amdgpu_buffer_rsrc_t rsrc_t;

constexpr int S = 2048, D = 512, F = 1376;
constexpr int GT = 88;
constexpr int WS_SA = 0;
constexpr int WS_SB = 32768;
constexpr int WS_ZP = 65536;
constexpr int WS_KVP = 131072;
constexpr int WS_G = 1 << 20;
constexpr int WS_O = 3 << 20;
constexpr int WS_PQ = 5 << 20;
constexpr int WS_BYTES = 9 << 20;

#define STAMP(i)
struct Params {
  const void *x, *wqkv, *wphi, *wo, *w1, *b1, *w2, *b2;
  void *out, *ws;
};

DI rsrc_t mk(const void* p, int bytes) { return __builtin_amdgcn_make_buffer_rsrc((void*)p, 0, bytes, 0x00020000); }
DI void dma16(rsrc_t r, unsigned voff, unsigned soff, unsigned lds) {
  asm volatile("s_mov_b32 m0, %0\n\ts_nop 0\n\tbuffer_load_dwordx4 %1, %2, %3 offen lds" ::"s"(lds), "v"(voff), "s"(r), "s"(soff) : "memory");
}
template <int N> DI void wait_vm_bar() { asm volatile("s_waitcnt vmcnt(%0) lgkmcnt(0)\n\ts_barrier" ::"i"(N) : "memory"); }
DI void bar() { asm volatile("s_waitcnt lgkmcnt(0)\n\ts_barrier" ::: "memory"); }
DI u32x4 ld16(rsrc_t r, unsigned voff, unsigned soff) { return __builtin_amdgcn_raw_buffer_load_b128(r, voff, soff, 0); }
DI void st16(u32x4 v, rsrc_t r, unsigned voff, unsigned soff) { __builtin_amdgcn_raw_buffer_store_b128(v, r, voff, soff, 0); }
DI s16x4 trd(unsigned a) { return __builtin_amdgcn_ds_read_tr16_b64_v4i16((LAS s16x4*)a); }
DI bf16x8 trd2(unsigned a0, unsigned a1) {
  s16x4 lo = trd(a0), hi = trd(a1);
  return __builtin_shufflevector(lo, hi, 0, 1, 2, 3, 4, 5, 6, 7);
}
DI bf16x8 lds16(unsigned a) { return *(LAS bf16x8*)a; }
DI f32x16 mfma(bf16x8 a, bf16x8 b, f32x16 c) { return __builtin_amdgcn_mfma_f32_32x32x16_bf16(a, b, c, 0, 0, 0); }
DI unsigned pk(float lo, float hi) {
  f32x2 v = {lo, hi};
  return __builtin_bit_cast(unsigned, __builtin_convertvector(v, bfx2));
}
DI float asf(unsigned u) { return __builtin_bit_cast(float, u); }
DI unsigned asu(float u) { return __builtin_bit_cast(unsigned, u); }
DI float blo(unsigned u) { return __builtin_bit_cast(float, u << 16); }
DI float bhi(unsigned u) { return __builtin_bit_cast(float, u & 0xffff0000u); }
DI bf16x8 pack8(const f32x16& x, int s) {
  u32x4 p = {pk(x[8 * s], x[8 * s + 1]), pk(x[8 * s + 2], x[8 * s + 3]), pk(x[8 * s + 4], x[8 * s + 5]), pk(x[8 * s + 6], x[8 * s + 7])};
  return __builtin_bit_cast(bf16x8, p);
}
DI float dot2(unsigned a, unsigned b, float c) { return __builtin_amdgcn_fdot2_f32_bf16(__builtin_bit_cast(bfx2, a), __builtin_bit_cast(bfx2, b), c, false); }
DI float xhalf(float v) {
  unsigned u = __builtin_bit_cast(unsigned, v);
  auto r = __builtin_amdgcn_permlane32_swap(u, u, false, false);
  return __builtin_bit_cast(float, r[0] ^ r[1] ^ u);
}
DI float half_max(float v) {
  unsigned u = __builtin_bit_cast(unsigned, v);
  auto r = __builtin_amdgcn_permlane32_swap(u, u, false, false);
  return fmaxf(asf(r[0]), asf(r[1]));
}
DI float half_sum(float v) {
  unsigned u = __builtin_bit_cast(unsigned, v);
  auto r = __builtin_amdgcn_permlane32_swap(u, u, false, false);
  return asf(r[0]) + asf(r[1]);
}
template <int C> DI float dpp_add(float v) { return v + __builtin_amdgcn_update_dpp(0.f, v, C, 0xf, 0xf, false); }
DI float wave_sum(float v) {
  v = dpp_add<0x128>(v);
  v = dpp_add<0x124>(v);
  v = dpp_add<0x122>(v);
  v = dpp_add<0x121>(v);
  unsigned u = __builtin_bit_cast(unsigned, v);
  auto r = __builtin_amdgcn_permlane16_swap(u, u, false, false);
  v = asf(r[0]) + asf(r[1]);
  return half_sum(v);
}

DI void stats_phase(const Params& p, unsigned lds, int b, unsigned tag) {
  const int t = threadIdx.x, l = t & 63, w = t >> 6;
  rsrc_t rx = mk(p.x, S * D * 2), rw = mk(p.ws, WS_BYTES);
  u32x4 v = ld16(rx, t * 16, b * 8192);
  float e[8];
#pragma unroll
  for (int i = 0; i < 4; i++) { e[2 * i] = blo(v[i]); e[2 * i + 1] = bhi(v[i]); }
  float s = ((e[0] + e[1]) + (e[2] + e[3])) + ((e[4] + e[5]) + (e[6] + e[7]));
  const float m = wave_sum(s) * (1.0f / D);
  float q = 0.f;
#pragma unroll
  for (int i = 0; i < 8; i++) { e[i] -= m; q += e[i] * e[i]; }
  const float r = __builtin_amdgcn_rsqf(wave_sum(q) * (1.0f / D) + 1e-5f);
#pragma unroll
  for (int i = 0; i < 8; i++) e[i] *= r;
  s = ((e[0] + e[1]) + (e[2] + e[3])) + ((e[4] + e[5]) + (e[6] + e[7]));
  const float m2 = wave_sum(s) * (1.0f / D);
  q = 0.f;
#pragma unroll
  for (int i = 0; i < 8; i++) { float d = e[i] - m2; q += d * d; }
  const float r2 = __builtin_amdgcn_rsqf(wave_sum(q) * (1.0f / D) + 1e-5f);
  const float a = r * r2, bb = -(m * r + m2) * r2;
  if (l < 2) {
    u32x4 g = {__builtin_bit_cast(unsigned, l ? r : a), tag, __builtin_bit_cast(unsigned, l ? m : bb), tag};
    *(LAS u32x4*)(lds + l * 128 + w * 16) = g;
  }
  bar();
  if (t < 16) {
    u32x4 g = *(LAS u32x4*)(lds + t * 16);
    st16(g, rw, (t >> 3) * 32768 + (t & 7) * 16, WS_SA + b * 128);
  }
}

constexpr int G1_W = 0, G1_WS = 24576, G1_X = 4 * G1_WS, G1_XS = 16384, G1_ST = G1_X + 2 * G1_XS;
constexpr int G1_LDS = G1_ST + 16384;
DI void g1_phase(const Params& p, unsigned lds, int dt, int ft, int rot) {
  const int t = threadIdx.x, l = t & 63, w = __builtin_amdgcn_readfirstlane(t >> 6);
  rsrc_t rx = mk(p.x, S * D * 2), rw1 = mk(p.w1, S * F * 2), rw = mk(p.ws, WS_BYTES);
  unsigned wv[3];
#pragma unroll
  for (int i = 0; i < 3; i++) {
    unsigned u = (w * 3 + i) * 64 + l;
    unsigned row = u / 12, ch = u - row * 12;
    if (ft * 96 + ch * 8 >= F) ch = 0;
    wv[i] = row * (F * 2) + ch * 16;
  }
  const unsigned xv = (t >> 3) * 1024 + (t & 7) * 16;
  auto issue_w = [&](int s) {
    unsigned sidx = (s + rot) & 15;
    unsigned so = sidx * (128 * F * 2) + ft * 192;
#pragma unroll
    for (int i = 0; i < 3; i++) dma16(rw1, wv[i], so, lds + G1_W + (s & 3) * G1_WS + (w * 3 + i) * 1024);
  };
  u32x4 xr[16][2];
  auto issue_x = [&](int s) {
    unsigned sidx = (s + rot) & 15;
#pragma unroll
    for (int i = 0; i < 2; i++) xr[s][i] = ld16(rx, xv, sidx * (128 * 1024) + i * 65536 + dt * 128);
  };
#pragma unroll
  for (int s = 0; s < 4; s++) { issue_x(s); __builtin_amdgcn_sched_barrier(0); issue_w(s); __builtin_amdgcn_sched_barrier(0); }
  {
    u32x4 g[4];
#pragma unroll
    for (int i = 0; i < 4; i++) g[i] = ld16(rw, t * 16, WS_SA + i * 8192);
    __builtin_amdgcn_sched_barrier(0);
#pragma unroll
    for (int s = 4; s < 16; s++) issue_x(s);
    __builtin_amdgcn_sched_barrier(0);
#pragma unroll
    for (int i = 0; i < 4; i++) { u32x2 ab = {g[i][0], g[i][2]}; *(LAS u32x2*)(lds + G1_ST + (i * 512 + t) * 8) = ab; }
  }
  bar();
  const unsigned xw = lds + G1_X + (t >> 3) * 128 + (((t & 7) ^ (((t >> 4) & 1) << 2)) * 16);
  auto norm_x = [&](int s) {
    unsigned sidx = (s + rot) & 15;
#pragma unroll
    for (int i = 0; i < 2; i++) {
      f32x2 ab = *(LAS f32x2*)(lds + G1_ST + (sidx * 128 + i * 64 + (t >> 3)) * 8);
      u32x4 v = xr[s][i], o;
#pragma unroll
      for (int j = 0; j < 4; j++) o[j] = pk(blo(v[j]) * ab[0] + ab[1], bhi(v[j]) * ab[0] + ab[1]);
      *(LAS u32x4*)(xw + (s & 1) * G1_XS + i * 8192) = o;
    }
  };
  STAMP(3);
  norm_x(0);
  const int h = l >> 5, blk = (l >> 4) & 1, q = (l & 15) >> 2, pp = l & 3;
  const int nf = w % 3, md = (w / 3) & 1;
  const unsigned aaddr = lds + G1_W + (8 * h + q) * 192 + nf * 64 + blk * 32 + pp * 8;
  const unsigned baddr = lds + G1_X + (8 * h + q) * 128 + (((md * 4 + blk * 2 + (pp >> 1)) ^ (((q >> 1) & 1) << 2)) * 16) + (pp & 1) * 8;
  f32x16 acc = {};
#pragma unroll
  for (int s = 0; s < 16; s++) {
    if (s == 0) wait_vm_bar<43>();
    else if (s == 1) wait_vm_bar<38>();
    else if (s == 2) wait_vm_bar<36>();
    else if (s == 3) wait_vm_bar<34>();
    else if (s < 14) wait_vm_bar<6>();
    else if (s == 14) wait_vm_bar<3>();
    else wait_vm_bar<0>();
    if (s >= 1 && s + 3 < 16) issue_w(s + 3);
    if (s + 1 < 16) norm_x(s + 1);
    if (w < 6) {
#pragma unroll
      for (int ks = 0; ks < 8; ks++) {
        bf16x8 a = trd2(aaddr + (s & 3) * G1_WS + ks * 16 * 192, aaddr + (s & 3) * G1_WS + ks * 16 * 192 + 4 * 192);
        bf16x8 b = trd2(baddr + (s & 1) * G1_XS + ks * 2048, baddr + (s & 1) * G1_XS + ks * 2048 + 512);
        acc = mfma(a, b, acc);
      }
    }
  }
  STAMP(4);
  if (w < 6) {
    rsrc_t rb1 = mk(p.b1, F * 2);
    const int fb = ft * 96 + nf * 32;
    if (fb < GT * 16) {
#pragma unroll
      for (int g = 0; g < 4; g++) {
        u32x2 bv = __builtin_amdgcn_raw_buffer_load_b64(rb1, (8 * g + 4 * h) * 2, fb * 2, 0);
        float bs[4] = {blo(bv[0]), bhi(bv[0]), blo(bv[1]), bhi(bv[1])};
#pragma unroll
        for (int j = 0; j < 4; j++) {
          float u = acc[4 * g + j] + bs[j];
          float e = __builtin_amdgcn_exp2f(-2.302208198f * u * (1.0f + 0.044715f * u * u));
          float r = u * __builtin_amdgcn_rcpf(1.0f + e);
          acc[4 * g + j] = fb < F ? r : 0.f;
        }
      }
      const unsigned so = WS_G + ((dt * 2 + md) * GT + ft * 6 + nf * 2) * 1024;
      st16(__builtin_bit_cast(u32x4, pack8(acc, 0)), rw, l * 16, so);
      st16(__builtin_bit_cast(u32x4, pack8(acc, 1)), rw, l * 16 + 1024, so);
    }
  }
}

constexpr int G2_SLOT = 40960, G2_XO = 0, G2_WO = 16384, G2_PHI = 3 * G2_SLOT, G2_ONES = G2_PHI + 2048, G2_CSP = G2_ONES + 64, G2_CS = G2_CSP + 6144,
              G2_LDS = G2_CS + 768;
constexpr int G2_PK = 0, G2_V = 32768, G2_T = 49152;

DI void dual_softmax(f32x16& p, float sc) {
  float mx0 = fmaxf(fmaxf(fmaxf(p[0], p[1]), fmaxf(p[2], p[3])), fmaxf(fmaxf(p[4], p[5]), fmaxf(p[6], p[7])));
  float mx1 = fmaxf(fmaxf(fmaxf(p[8], p[9]), fmaxf(p[10], p[11])), fmaxf(fmaxf(p[12], p[13]), fmaxf(p[14], p[15])));
  mx0 = half_max(mx0) * sc;
  mx1 = half_max(mx1) * sc;
  float s0 = 0.f, s1 = 0.f;
#pragma unroll
  for (int i = 0; i < 8; i++) {
    p[i] = __builtin_amdgcn_exp2f(p[i] * sc - mx0);
    p[8 + i] = __builtin_amdgcn_exp2f(p[8 + i] * sc - mx1);
    s0 += p[i];
    s1 += p[8 + i];
  }
  s0 = __builtin_amdgcn_rcpf(half_sum(s0));
  s1 = __builtin_amdgcn_rcpf(half_sum(s1));
#pragma unroll
  for (int i = 0; i < 8; i++) { p[i] *= s0; p[8 + i] *= s1; }
}

DI void g2a_phase(const Params& p, unsigned lds, int rt, int hg, int rot, bf16x8 (&phq)[2][2]) {
  const int t = threadIdx.x, l = t & 63, w = __builtin_amdgcn_readfirstlane(t >> 6);
  rsrc_t rx = mk(p.x, S * D * 2), rq = mk(p.wqkv, D * 3 * D * 2), rw = mk(p.ws, WS_BYTES), rp = mk(p.wphi, 32 * 256 * 2);
  unsigned xv[2];
#pragma unroll
  for (int i = 0; i < 2; i++) {
    unsigned row = (w * 2 + i) * 8 + (l >> 3);
    xv[i] = row * 1024 + (((l & 7) ^ ((row >> 1) & 7)) * 16);
  }
  const unsigned wk = w * 8 + (l >> 3);
  const unsigned wv = wk * 3072 + (((l & 7) ^ (((wk >> 1) & 1) << 2)) * 16);
  auto issue = [&](int s, int slot) {
    unsigned sidx = (s + rot) & 7;
    unsigned base = lds + slot * G2_SLOT;
#pragma unroll
    for (int i = 0; i < 2; i++) dma16(rx, xv[i], rt * (128 * 1024) + sidx * 128, base + G2_XO + (w * 2 + i) * 1024);
#pragma unroll
    for (int i = 0; i < 3; i++) dma16(rq, wv, sidx * (64 * 3072) + i * 1024 + hg * 128, base + G2_WO + i * 8192 + w * 1024);
  };
  if (w < 2) dma16(rp, l * 16 + w * 1024, hg * 2048, lds + G2_PHI + w * 1024);
  issue(0, 0);
  issue(1, 1);
  if (t < 16) *(LAS unsigned*)(lds + G2_ONES + t * 4) = 0x3f803f80u;
  const int h = l >> 5, blk = (l >> 4) & 1, q = (l & 15) >> 2, pp = l & 3;
  const int mq = w >> 1, hp = w & 1;
  const unsigned xrow = mq * 32 + (l & 31);
  const unsigned xaddr = lds + G2_XO + xrow * 128 + ((h ^ ((xrow >> 1) & 7)) * 16);
  const unsigned waddr = lds + G2_WO + (8 * h + q) * 128 + (((hp * 4 + blk * 2 + (pp >> 1)) ^ (((q >> 1) & 1) << 2)) * 16) + (pp & 1) * 8;
  f32x16 acc[3] = {};
  float cs[3] = {0.f, 0.f, 0.f};
  int slot = 0;
  for (int s = 0; s < 8; s++) {
    if (s < 7) wait_vm_bar<5>(); else wait_vm_bar<0>();
    if (s + 2 < 8) issue(s + 2, slot == 0 ? 2 : slot - 1);
    const unsigned sb = slot * G2_SLOT;
#pragma unroll
    for (int ks = 0; ks < 4; ks++) {
      bf16x8 xb = lds16((xaddr ^ (ks << 5)) + sb);
      bf16x8 wa[3];
#pragma unroll
      for (int i = 0; i < 3; i++) wa[i] = trd2(waddr + sb + i * 8192 + ks * 2048, waddr + sb + i * 8192 + ks * 2048 + 512);
#pragma unroll
      for (int i = 0; i < 3; i++) acc[i] = mfma(wa[i], xb, acc[i]);
      if (ks == mq) {
#pragma unroll
        for (int i = 0; i < 3; i++) {
          u32x4 u = __builtin_bit_cast(u32x4, wa[i]);
#pragma unroll
          for (int j = 0; j < 4; j++) cs[i] = dot2(u[j], 0x3f803f80u, cs[i]);
        }
      }
    }
    slot = slot == 2 ? 0 : slot + 1;
  }
  STAMP(4);
  u32x4 sg = ld16(rw, xrow * 16, WS_SB + rt * 2048);
#pragma unroll
  for (int i = 0; i < 3; i++) *(LAS float*)(lds + G2_CSP + ((hp * 3 + i) * 32 + (l & 31)) * 32 + (mq * 2 + h) * 4) = cs[i];
  bar();
  if (t < 192) {
    f32x4 a = *(LAS f32x4*)(lds + G2_CSP + t * 32), b = *(LAS f32x4*)(lds + G2_CSP + t * 32 + 16);
    *(LAS float*)(lds + G2_CS + t * 4) = ((a[0] + a[1]) + (a[2] + a[3])) + ((b[0] + b[1]) + (b[2] + b[3]));
  }
  bar();
  const float r = asf(sg[0]), m = asf(sg[2]);
#pragma unroll
  for (int i = 0; i < 3; i++)
#pragma unroll
    for (int g = 0; g < 4; g++) {
      f32x4 c = *(LAS f32x4*)(lds + G2_CS + ((hp * 3 + i) * 32 + 8 * g + 4 * h) * 4);
#pragma unroll
      for (int j = 0; j < 4; j++) {
        float v = acc[i][4 * g + j] - m * c[j];
        acc[i][4 * g + j] = i == 2 ? v * r : v;
      }
    }
  STAMP(5);
  bf16x8 wf[2];
#pragma unroll
  for (int e = 0; e < 2; e++) {
    unsigned a = lds + G2_PHI + (hp * 2 + e) * 512 + (4 * h + q) * 32 + pp * 8;
    u32x4 u = __builtin_bit_cast(u32x4, trd2(a, a + 8 * 32));
    unsigned sgn = blk ? 0x80008000u : 0u;
#pragma unroll
    for (int j = 0; j < 4; j++) u[j] ^= sgn;
    wf[e] = __builtin_bit_cast(bf16x8, u);
  }
  const float sc = r * 1.4426950408889634f;
  const unsigned fpk = ((xrow & 3) << 3) | ((xrow >> 1) & 7), fv = ((xrow << 1) | ((xrow >> 3) & 1)) & 15;
#pragma unroll
  for (int g = 0; g < 4; g++) {
    u32x2 u = {pk(acc[2][4 * g], acc[2][4 * g + 1]), pk(acc[2][4 * g + 2], acc[2][4 * g + 3])};
    *(LAS u32x2*)(lds + G2_V + xrow * 128 + (((hp * 8 + 2 * g + h) ^ fv) * 8)) = u;
  }
#pragma unroll
  for (int e = 0; e < 2; e++) {
    f32x16 z = {};
    f32x16 pkv = mfma(wf[e], pack8(acc[1], e), z);
    dual_softmax(pkv, sc);
#pragma unroll
    for (int g = 0; g < 4; g++) {
      u32x2 u = {pk(pkv[4 * g], pkv[4 * g + 1]), pk(pkv[4 * g + 2], pkv[4 * g + 3])};
      *(LAS u32x2*)(lds + G2_PK + xrow * 256 + ((((hp * 2 + e) * 8 + 2 * g + h) ^ fpk) * 8)) = u;
    }
  }
  bar();
  if (w < 4) {
    unsigned aa[2], ba[2];
#pragma unroll
    for (int tt = 0; tt < 2; tt++) {
      unsigned sr = 8 * h + 4 * tt + q;
      unsigned f1 = ((sr & 3) << 3) | ((sr >> 1) & 7), f2 = ((sr << 1) | ((sr >> 3) & 1)) & 15;
      aa[tt] = lds + G2_PK + sr * 256 + (((w * 8 + blk * 4 + pp) ^ f1) * 8);
      ba[tt] = lds + G2_V + sr * 128 + (((w * 4 + pp) ^ f2) * 8);
    }
    f32x16 kv = {};
#pragma unroll
    for (int ks = 0; ks < 8; ks++) {
      bf16x8 a = trd2(aa[0] + ks * 4096, aa[1] + ks * 4096);
      unsigned b0 = blk ? lds + G2_ONES + pp * 8 : ba[0] + ks * 2048, b1 = blk ? lds + G2_ONES + pp * 8 : ba[1] + ks * 2048;
      bf16x8 b = trd2(b0, b1);
      kv = mfma(a, b, kv);
    }
    const int head = hg * 4 + w, c = l & 31;
    if (c <= 16) {
#pragma unroll
      for (int e = 0; e < 2; e++) {
        unsigned off = c < 16 ? WS_KVP + (rt * 32 + head) * 1024 + (h * 32 + e * 16 + c) * 16 : WS_ZP + head * 1024 + (h * 32 + e * 16 + rt) * 16;
        st16(__builtin_bit_cast(u32x4, pack8(kv, e)), rw, off, 0);
      }
    }
  }
  STAMP(6);
#pragma unroll
  for (int e = 0; e < 2; e++) {
    f32x16 z = {};
    f32x16 pq = mfma(wf[e], pack8(acc[0], e), z);
    dual_softmax(pq, sc);
    phq[e][0] = pack8(pq, 0);
    phq[e][1] = pack8(pq, 1);
  }
}

DI void g2b_phase(const Params& p, unsigned lds, int rt, int hg, bf16x8 (&phq)[2][2]) {
  const int t = threadIdx.x, l = t & 63, w = __builtin_amdgcn_readfirstlane(t >> 6);
  rsrc_t rw = mk(p.ws, WS_BYTES);
  const int hh = w & 3, half = w >> 2, head = hg * 4 + hh;
  u32x4 v[8];
#pragma unroll
  for (int i = 0; i < 8; i++) v[i] = ld16(rw, l * 16 + ((half * 8 + i) * 32 + head) * 1024, WS_KVP);
  u32x4 zv = {0, 0, 0, 0};
  if (half == 0) zv = ld16(rw, l * 16 + head * 1024, WS_ZP);
  float s[8] = {};
  unsigned sel_lo = 0x00003f80u;
  asm("" : "+s"(sel_lo));
#pragma unroll
  for (int i = 0; i < 8; i++)
#pragma unroll
    for (int j = 0; j < 4; j++) { s[2 * j] = dot2(v[i][j], sel_lo, s[2 * j]); s[2 * j + 1] = dot2(v[i][j], 0x3f800000u, s[2 * j + 1]); }
  u32x4 o = {pk(s[0], s[1]), pk(s[2], s[3]), pk(s[4], s[5]), pk(s[6], s[7])};
  const unsigned ta = lds + G2_T + (half * 4 + hh) * 2048 + ((l >> 4) & 1) * 1024 + ((l >> 5) * 32 + (l & 15)) * 16;
  *(LAS u32x4*)ta = o;
  *(LAS u32x4*)(ta + 256) = zv;
  bar();
  const int mq = w >> 1, hp = w & 1;
#pragma unroll
  for (int e = 0; e < 2; e++) {
    f32x16 a = {};
#pragma unroll
    for (int c = 0; c < 2; c++)
#pragma unroll
      for (int sp = 0; sp < 2; sp++) a = mfma(lds16(lds + G2_T + (c * 4 + hp * 2 + e) * 2048 + sp * 1024 + l * 16), phq[e][sp], a);
    float den = ((a[8] + a[9]) + (a[10] + a[11])) + ((a[12] + a[13]) + (a[14] + a[15]));
    den = __builtin_amdgcn_rcpf(half_sum(den) + 1e-6f);
#pragma unroll
    for (int i = 0; i < 8; i++) a[i] *= den;
    st16(__builtin_bit_cast(u32x4, pack8(a, 0)), rw, l * 16 + ((rt * 4 + mq) * 32 + hg * 4 + hp * 2 + e) * 1024, WS_O);
  }
}

constexpr int P2_SLOT = 32768, P2_LO = 16384, P2_LDS = 4 * P2_SLOT;
DI void p2_phase(const Params& p, unsigned lds, int st, int dt) {
  const int t = threadIdx.x, l = t & 63, w = __builtin_amdgcn_readfirstlane(t >> 6);
  rsrc_t rx = mk(p.x, S * D * 2), rw2 = mk(p.w2, F * S * 2), rwo = mk(p.wo, D * D * 2), rw = mk(p.ws, WS_BYTES), rb2 = mk(p.b2, S * 2), ro = mk(p.out, S * D * 2);
  const unsigned trow = w * 16 + (l >> 3);
  const unsigned tch = ((l & 7) ^ (((trow >> 1) & 1) << 2)) * 16;
  const unsigned tv2 = trow * 4096 + tch, tvo = trow * 1024 + tch;
  const unsigned lvg = l * 16 + ((w >> 2) * GT + ((2 * w) & 7)) * 1024, lvo = l * 16 + ((w >> 2) * 32 + ((2 * w) & 7)) * 1024;
  auto issue = [&](int s) {
    unsigned base = lds + (s & 3) * P2_SLOT + w * 2048;
    if (s < 11) {
      dma16(rw2, tv2, s * (128 * 4096) + st * 128, base);
      dma16(rw2, tv2, s * (128 * 4096) + st * 128 + 8 * 4096, base + 1024);
      dma16(rw, lvg, WS_G + (dt * 2 * GT + s * 8) * 1024, base + P2_LO);
      dma16(rw, lvg, WS_G + (dt * 2 * GT + s * 8 + 1) * 1024, base + P2_LO + 1024);
    } else {
      int c = s - 11;
      dma16(rwo, tvo, c * (128 * 1024) + dt * 128, base);
      dma16(rwo, tvo, c * (128 * 1024) + dt * 128 + 8 * 1024, base + 1024);
      dma16(rw, lvo, WS_O + (st * 2 * 32 + c * 8) * 1024, base + P2_LO);
      dma16(rw, lvo, WS_O + (st * 2 * 32 + c * 8 + 1) * 1024, base + P2_LO + 1024);
    }
  };
  const unsigned ov = (t >> 3) * 1024 + (t & 7) * 16, os = st * 65536 + dt * 128;
  u32x4 xres = ld16(rx, ov, os);
  unsigned short b2v = __builtin_amdgcn_raw_buffer_load_b16(rb2, (t >> 3) * 2, st * 128, 0);
  __builtin_amdgcn_sched_barrier(0);
  issue(0);
  issue(1);
  issue(2);
  const int h = l >> 5, blk = (l >> 4) & 1, q = (l & 15) >> 2, pp = l & 3;
  const int kh = w >> 2, ms = (w >> 1) & 1, nd = w & 1;
  const unsigned tb = lds + kh * 8192 + (4 * h + q) * 128 + (pp & 1) * 8;
  const unsigned sw = ((q >> 1) & 1) << 2;
  const unsigned tu = tb + (((ms * 4 + blk * 2 + (pp >> 1)) ^ sw) * 16), to = tb + (((nd * 4 + blk * 2 + (pp >> 1)) ^ sw) * 16);
  const unsigned lu = lds + P2_LO + nd * 8192 + kh * 4096 + l * 16, lo = lds + P2_LO + ms * 8192 + kh * 4096 + l * 16;
  f32x16 acc = {};
  for (int s = 0; s < 11; s++) {
    wait_vm_bar<8>();
    issue(s + 3);
    const unsigned sb = (s & 3) * P2_SLOT;
#pragma unroll
    for (int ks = 0; ks < 4; ks++) {
      bf16x8 b = trd2(tu + sb + ks * 2048, tu + sb + ks * 2048 + 1024);
      bf16x8 a = lds16(lu + sb + ks * 1024);
      acc = mfma(a, b, acc);
    }
  }
  STAMP(11);
#pragma unroll
  for (int s = 11; s < 15; s++) {
    if (s < 13) wait_vm_bar<8>(); else if (s == 13) wait_vm_bar<4>(); else wait_vm_bar<0>();
    if (s + 3 < 15) issue(s + 3);
    const unsigned sb = (s & 3) * P2_SLOT;
#pragma unroll
    for (int ks = 0; ks < 4; ks++) {
      bf16x8 a = trd2(to + sb + ks * 2048, to + sb + ks * 2048 + 1024);
      bf16x8 b = lds16(lo + sb + ks * 1024);
      acc = mfma(a, b, acc);
    }
  }
  STAMP(12);
  const unsigned rb = lds;
#pragma unroll
  for (int g = 0; g < 4; g++) {
    f32x4 v = {acc[4 * g], acc[4 * g + 1], acc[4 * g + 2], acc[4 * g + 3]};
    *(LAS f32x4*)(rb + kh * 17408 + (ms * 32 + (l & 31)) * 272 + (nd * 32 + 8 * g + 4 * h) * 4) = v;
  }
  bar();
  {
    const unsigned ra = rb + (t >> 3) * 272 + (t & 7) * 32;
    f32x4 a0 = *(LAS f32x4*)ra, a1 = *(LAS f32x4*)(ra + 16), c0 = *(LAS f32x4*)(ra + 17408), c1 = *(LAS f32x4*)(ra + 17408 + 16);
    const float bb = blo(b2v);
    a0 += c0;
    a1 += c1;
    u32x4 o = {pk(a0[0] + bb + blo(xres[0]), a0[1] + bb + bhi(xres[0])), pk(a0[2] + bb + blo(xres[1]), a0[3] + bb + bhi(xres[1])),
               pk(a1[0] + bb + blo(xres[2]), a1[1] + bb + bhi(xres[2])), pk(a1[2] + bb + blo(xres[3]), a1[3] + bb + bhi(xres[3]))};
    st16(o, ro, ov, os);
  }
}

extern __shared__ __attribute__((aligned(128))) char smem[];
DI unsigned lds_base() { return (unsigned)(size_t)(LAS char*)smem; }

__global__ __launch_bounds__(512, 2) void k_stats(Params p) { STAMP(0); stats_phase(p, lds_base(), blockIdx.x, 1u); STAMP(1); }
__global__ __launch_bounds__(512, 2) void k_p1(Params p) {
  const int b = blockIdx.x, xc = b & 7, i = b >> 3;
  STAMP(2);
  if (i < 16) {
    const int ft = xc * 2 + (i >> 3);
    if (ft < 15) g1_phase(p, lds_base(), i & 7, ft, xc * 2);
  } else {
    bf16x8 phq[2][2];
    const int rt = i - 16, hg = xc;
    g2a_phase(p, lds_base(), rt, hg, xc, phq);
    rsrc_t rw = mk(p.ws, WS_BYTES);
#pragma unroll
    for (int e = 0; e < 4; e++) st16(__builtin_bit_cast(u32x4, phq[e >> 1][e & 1]), rw, threadIdx.x * 16 + e * 8192, WS_PQ + (rt * 8 + hg) * 32768);
  }
  STAMP(7);
}
__global__ __launch_bounds__(512, 2) void k_o(Params p) {
  const int b = blockIdx.x, hg = b & 7, rt = b >> 3;
  STAMP(8);
  bf16x8 phq[2][2];
  rsrc_t rw = mk(p.ws, WS_BYTES);
#pragma unroll
  for (int e = 0; e < 4; e++) phq[e >> 1][e & 1] = __builtin_bit_cast(bf16x8, ld16(rw, threadIdx.x * 16 + e * 8192, WS_PQ + (rt * 8 + hg) * 32768));
  g2b_phase(p, lds_base(), rt, hg, phq);
  STAMP(9);
}
__global__ __launch_bounds__(512, 2) void k_p2(Params p) {
  const int b = blockIdx.x, xc = b & 7, i = b >> 3;
  STAMP(10);
  p2_phase(p, lds_base(), xc * 4 + (i >> 3), i & 7);
  STAMP(13);
}
}

extern "C" void kernel_launch(void* const* d_in, const int* in_sizes, int n_in, void* d_out, int out_size, void* d_ws, size_t ws_size,
                              hipStream_t stream) {
  using namespace hk;
  Params p{d_in[0], d_in[1], d_in[2], d_in[3], d_in[5], d_in[6], d_in[7], d_in[8], d_out, d_ws};
  constexpr int L1 = G1_LDS > G2_LDS ? G1_LDS : G2_LDS;
  hipFuncSetAttribute((const void*)k_p1, hipFuncAttributeMaxDynamicSharedMemorySize, L1);
  hipFuncSetAttribute((const void*)k_o, hipFuncAttributeMaxDynamicSharedMemorySize, 65536);
  hipFuncSetAttribute((const void*)k_p2, hipFuncAttributeMaxDynamicSharedMemorySize, P2_LDS);
  k_stats<<<256, 512, 256, stream>>>(p);
  k_p1<<<256, 512, L1, stream>>>(p);
  k_o<<<128, 512, 65536, stream>>>(p);
  k_p2<<<256, 512, P2_LDS, stream>>>(p);
}
```

```cpp
#include <hip/hip_runtime.h>
#include <hip/hip_bf16.h>
#define FUSE_P2 1
#define CODE_BYTES 0

namespace hk {
#define DI __device__ __forceinline__
#define LAS __attribute__((address_space(3)))
typedef short bf16x8 __attribute__((ext_vector_type(8)));
typedef short s16x4 __attribute__((ext_vector_type(4)));
typedef float f32x16 __attribute__((ext_vector_type(16)));
typedef float f32x4 __attribute__((ext_vector_type(4)));
typedef float f32x2 __attribute__((ext_vector_type(2)));
typedef unsigned u32x4 __attribute__((ext_vector_type(4)));
typedef unsigned u32x2 __attribute__((ext_vector_type(2)));
typedef __bf16 bfx2 __attribute__((ext_vector_type(2)));
typedef __amdgpu_buffer_rsrc_t rsrc_t;

constexpr int S = 2048, D = 512, F = 1376;
constexpr int GT = 88;
constexpr int WS_SEED = 0;
constexpr int WS_F2 = 4096;
constexpr int WS_F3 = 20480;
constexpr int WS_F4 = 36864;
constexpr int WS_SB = 65536;
constexpr int WS_ZP = 98304;
constexpr int WS_KVP = 131072;
constexpr int WS_G = 1 << 20;
constexpr int WS_O = 3 << 20;
constexpr int WS_H = 5 << 20;
constexpr int WS_BYTES = 7 << 20;
constexpr int SC1 = 16;
constexpr int NT = 768;
constexpr unsigned OOB = 0x40000000u;
constexpr int SPIN_CAP = 1 << 14;

#define STAMP(i)
#define STAMPW(i, cond)
struct Params {
  const void *x, *wqkv, *wphi, *wo, *w1, *b1, *w2, *b2;
  void *out, *ws;
};

#define SB0() __builtin_amdgcn_sched_barrier(0)
DI rsrc_t mk(const void* p, int bytes) { return __builtin_amdgcn_make_buffer_rsrc((void*)p, 0, bytes, 0x00020000); }
DI void dma16(rsrc_t r, unsigned voff, unsigned soff, unsigned lds) {
  asm volatile("s_mov_b32 m0, %0\n\ts_nop 0\n\tbuffer_load_dwordx4 %1, %2, %3 offen lds" ::"s"(lds), "v"(voff), "s"(r), "s"(soff) : "memory");
}
DI void dma16x4_sc1(rsrc_t r, unsigned voff, unsigned soff, unsigned lds) {
  asm volatile("s_mov_b32 m0, %0\n\ts_nop 0\n\tbuffer_load_dwordx4 %1, %2, %3 offen sc1 lds\n\tbuffer_load_dwordx4 %1, %2, %3 offen offset:1024 sc1 lds\n\t"
               "buffer_load_dwordx4 %1, %2, %3 offen offset:2048 sc1 lds\n\tbuffer_load_dwordx4 %1, %2, %3 offen offset:3072 sc1 lds" ::"s"(lds), "v"(voff), "s"(r), "s"(soff) : "memory");
}
template <int N> DI void wvb() { asm volatile("s_waitcnt vmcnt(%0) lgkmcnt(0)\n\ts_barrier" ::"i"(N) : "memory"); }
DI void bar() { asm volatile("s_waitcnt lgkmcnt(0)\n\ts_barrier" ::: "memory"); }
DI void drain() { asm volatile("s_waitcnt vmcnt(0)" ::: "memory"); }
template <int AUX = 0> DI u32x4 ld16(rsrc_t r, unsigned voff, unsigned soff) { return __builtin_amdgcn_raw_buffer_load_b128(r, voff, soff, AUX); }
template <int AUX = 0> DI u32x2 ld8(rsrc_t r, unsigned voff, unsigned soff) { return __builtin_amdgcn_raw_buffer_load_b64(r, voff, soff, AUX); }
template <int AUX = 0> DI void st16(u32x4 v, rsrc_t r, unsigned voff) { __builtin_amdgcn_raw_buffer_store_b128(v, r, voff, 0, AUX); }
DI s16x4 trd(unsigned a) { return __builtin_amdgcn_ds_read_tr16_b64_v4i16((LAS s16x4*)a); }
DI bf16x8 trd2(unsigned a0, unsigned a1) {
  s16x4 lo = trd(a0), hi = trd(a1);
  return __builtin_shufflevector(lo, hi, 0, 1, 2, 3, 4, 5, 6, 7);
}
DI bf16x8 lds16(unsigned a) { return *(LAS bf16x8*)a; }
DI f32x16 mfma(bf16x8 a, bf16x8 b, f32x16 c) { return __builtin_amdgcn_mfma_f32_32x32x16_bf16(a, b, c, 0, 0, 0); }
DI unsigned pk(float lo, float hi) {
  f32x2 v = {lo, hi};
  return __builtin_bit_cast(unsigned, __builtin_convertvector(v, bfx2));
}
DI float asf(unsigned u) { return __builtin_bit_cast(float, u); }
DI unsigned asu(float u) { return __builtin_bit_cast(unsigned, u); }
DI float blo(unsigned u) { return asf(u << 16); }
DI float bhi(unsigned u) { return asf(u & 0xffff0000u); }
DI u32x4 pack8u(const f32x16& x, int s) {
  u32x4 p = {pk(x[8 * s], x[8 * s + 1]), pk(x[8 * s + 2], x[8 * s + 3]), pk(x[8 * s + 4], x[8 * s + 5]), pk(x[8 * s + 6], x[8 * s + 7])};
  return p;
}
DI bf16x8 pack8(const f32x16& x, int s) { return __builtin_bit_cast(bf16x8, pack8u(x, s)); }
DI float dot2(unsigned a, unsigned b, float c) { return __builtin_amdgcn_fdot2_f32_bf16(__builtin_bit_cast(bfx2, a), __builtin_bit_cast(bfx2, b), c, false); }
DI float half_sum(float v) {
  unsigned u = asu(v);
  auto r = __builtin_amdgcn_permlane32_swap(u, u, false, false);
  return asf(r[0]) + asf(r[1]);
}
template <int C> DI float dpp_add(float v) { return v + __builtin_amdgcn_update_dpp(0.f, v, C, 0xf, 0xf, false); }
DI float half32_sum(float v) {
  v = dpp_add<0x128>(v);
  v = dpp_add<0x124>(v);
  v = dpp_add<0x122>(v);
  v = dpp_add<0x121>(v);
  unsigned u = asu(v);
  auto r = __builtin_amdgcn_permlane16_swap(u, u, false, false);
  return asf(r[0]) + asf(r[1]);
}
DI void put_flag(rsrc_t rw, unsigned off, unsigned tag) {
  u32x2 f = {tag, ~tag};
  __builtin_amdgcn_raw_buffer_store_b64(f, rw, off, 0, SC1);
}
DI void poll_flags(rsrc_t rw, unsigned off, unsigned tag) {
#pragma nounroll
  for (int sp = 0; sp < SPIN_CAP; sp++) {
    asm volatile("" ::: "memory");
    u32x2 f = ld8<SC1>(rw, off, 0);
    if (__all(f[0] == tag && f[1] == ~tag)) break;
    __builtin_amdgcn_s_sleep(1);
  }
  asm volatile("" ::: "memory");
}
DI bool lds_arrive(unsigned cnt, unsigned n) {
  asm volatile("s_waitcnt lgkmcnt(0)" ::: "memory");
  unsigned old = 0;
  if ((threadIdx.x & 63) == 0) old = __hip_atomic_fetch_add((LAS unsigned*)cnt, 1u, __ATOMIC_RELAXED, __HIP_MEMORY_SCOPE_WORKGROUP);
  old = __builtin_amdgcn_readfirstlane(old);
  asm volatile("" ::: "memory");
  return old == n - 1;
}
DI void lds_wait(unsigned cnt, unsigned n) {
  while (__hip_atomic_load((LAS unsigned*)cnt, __ATOMIC_RELAXED, __HIP_MEMORY_SCOPE_WORKGROUP) < n) __builtin_amdgcn_s_sleep(0);
  asm volatile("" ::: "memory");
}

constexpr int L_CTL = 147456;
constexpr int L_MISC = L_CTL + 512;
constexpr int LDS_BYTES = L_MISC + 9216;
constexpr int G1_W = 0, G1_WS = 24576, G1_X = 4 * G1_WS, G1_XS = 16384;
constexpr int G2_SLOT = 40960, G2_XO = 0, G2_WO = 16384, G2_PHI = L_MISC, G2_ONES = G2_PHI + 2048, G2_CSP = G2_ONES + 64, G2_CS = G2_CSP + 6144;
constexpr int G2_PK = 98304, G2_V = G2_PK + 32768, G2_T = G2_PK;
constexpr int P2_T = 0, P2_L = 98304, P2_S = 16384;
static_assert(G2_CS + 768 <= LDS_BYTES && LDS_BYTES <= 163840, "lds");

DI void ln_phase(const Params& p, unsigned lds, int lb, int c, unsigned tag, u32x4 v0, u32x4 v1) {
  const int l = threadIdx.x & 63;
  rsrc_t rw = mk(p.ws, WS_BYTES);
  const unsigned img = lds + G1_X;
  float e[16];
#pragma unroll
  for (int i = 0; i < 4; i++) { e[2 * i] = blo(v0[i]); e[2 * i + 1] = bhi(v0[i]); e[8 + 2 * i] = blo(v1[i]); e[9 + 2 * i] = bhi(v1[i]); }
  float s = 0.f;
#pragma unroll
  for (int i = 0; i < 16; i += 2) s += e[i] + e[i + 1];
  const float m = half32_sum(s) * (1.0f / D);
  float q = 0.f, q2 = 0.f;
#pragma unroll
  for (int i = 0; i < 16; i += 2) { e[i] -= m; e[i + 1] -= m; q += e[i] * e[i]; q2 += e[i + 1] * e[i + 1]; }
  const float r = __builtin_amdgcn_rsqf(half32_sum(q + q2) * (1.0f / D) + 1e-5f);
  s = 0.f;
#pragma unroll
  for (int i = 0; i < 16; i += 2) { e[i] *= r; e[i + 1] *= r; s += e[i] + e[i + 1]; }
  const float m2 = half32_sum(s) * (1.0f / D);
  q = 0.f; q2 = 0.f;
#pragma unroll
  for (int i = 0; i < 16; i += 2) { e[i] -= m2; e[i + 1] -= m2; q += e[i] * e[i]; q2 += e[i + 1] * e[i + 1]; }
  const float r2 = __builtin_amdgcn_rsqf(half32_sum(q + q2) * (1.0f / D) + 1e-5f);
  u32x4 o0, o1;
#pragma unroll
  for (int i = 0; i < 4; i++) { o0[i] = pk(e[2 * i] * r2, e[2 * i + 1] * r2); o1[i] = pk(e[8 + 2 * i] * r2, e[9 + 2 * i] * r2); }
  const int row = c * 2 + (l >> 5);
  *(LAS u32x4*)(img + row * 1056 + (l & 31) * 16) = o0;
  *(LAS u32x4*)(img + row * 1056 + (l & 31) * 16 + 512) = o1;
  if ((l & 31) == 0) {
    u32x4 g = {asu(r), tag, asu(m), tag};
    *(LAS u32x4*)(lds + L_CTL + 64 + row * 16) = g;
  }
  lds_arrive(lds + L_CTL, 8);
  lds_wait(lds + L_CTL, 8);
#pragma unroll
  for (int i = 0; i < 2; i++) {
    const unsigned a = img + ((l >> 5) * 8 + ((l & 15) >> 2)) * 1056 + (2 * c + i) * 64 + ((l >> 4) & 1) * 32 + (l & 3) * 8;
    bf16x8 v = trd2(a, a + 4 * 1056);
    st16<SC1>(__builtin_bit_cast(u32x4, v), rw, WS_H + ((2 * c + i) * 128 + lb) * 1024 + l * 16);
  }
  drain();
  if (lds_arrive(lds + L_CTL + 4, 8)) {
    if (l < 16) {
      u32x4 g = *(LAS u32x4*)(lds + L_CTL + 64 + l * 16);
      st16<SC1>(g, rw, WS_SB + lb * 256 + l * 16);
    }
  }
}
DI void poll_ln(rsrc_t rw, unsigned tag) {
  const int l = threadIdx.x & 63;
#pragma nounroll
  for (int sp = 0; sp < SPIN_CAP; sp++) {
    asm volatile("" ::: "memory");
    u32x4 f0 = ld16<SC1>(rw, WS_SB + l * 256, 0), f1 = ld16<SC1>(rw, WS_SB + 16384 + l * 256, 0);
    if (__all(f0[1] == tag && f0[3] == tag && f1[1] == tag && f1[3] == tag)) break;
    __builtin_amdgcn_s_sleep(1);
  }
  asm volatile("" ::: "memory");
}

DI void g1_wloader(const Params& p, unsigned lds, int ft, int rot, int j) {
  const int l = threadIdx.x & 63;
  rsrc_t rw1 = mk(p.w1, S * F * 2);
  unsigned wv[3];
#pragma unroll
  for (int i = 0; i < 3; i++) {
    unsigned u = i * 64 + l;
    unsigned row = u / 12, ch = u - row * 12;
    wv[i] = (ft * 96 + ch * 8 >= F) ? OOB : (j * 32 + row) * (F * 2) + ch * 16;
  }
  const unsigned wl = lds + G1_W + j * 6144;
  auto issue = [&](int s) {
    const unsigned so = ((s + rot) & 15) * (128 * F * 2) + ft * 192, wd = wl + (s & 3) * G1_WS;
#pragma unroll
    for (int g = 0; g < 2; g++)
#pragma unroll
      for (int i = 0; i < 3; i++) dma16(rw1, wv[i], so + g * (16 * F * 2), wd + g * 3072 + i * 1024);
  };
#pragma nounroll
  for (int s = 0; s < 4; s++) issue(s);
  wvb<18>();
  wvb<12>();
  for (int s = 1; s < 13; s++) {
    issue(s + 3);
    wvb<12>();
  }
  wvb<6>();
  wvb<0>();
}
DI void g1_xloader(const Params& p, unsigned lds, int dt, int rot, int j, unsigned tag) {
  const int l = threadIdx.x & 63;
  rsrc_t rw = mk(p.ws, WS_BYTES);
  const unsigned xv = l * 16;
  auto issue = [&](int s, int slot) {
    const unsigned so = WS_H + ((dt * 2 + j) * 128 + ((s + rot) & 15) * 8) * 1024, xd = lds + G1_X + slot * G1_XS + j * 8192;
    dma16x4_sc1(rw, xv, so, xd);
    dma16x4_sc1(rw, xv, so + 4096, xd + 4096);
  };
  poll_ln(rw, tag);
  STAMPW(2, j == 0);
  issue(0, 0); issue(1, 1);
  wvb<8>();
  int slot = 2;
  for (int s = 0; s < 14; s++) {
    issue(s + 2, slot);
    slot = slot == 2 ? 0 : slot + 1;
    wvb<8>();
  }
  wvb<0>();
}
DI void g1_compute(const Params& p, unsigned lds, int dt, int ft, int c, unsigned tag) {
  const int l = threadIdx.x & 63;
  rsrc_t rw = mk(p.ws, WS_BYTES), rb1 = mk(p.b1, F * 2);
  const int h = l >> 5, blk = (l >> 4) & 1, q = (l & 15) >> 2, pp = l & 3;
  const int nf = c % 3, md = c / 3;
  const int fb = ft * 96 + nf * 32;
  u32x2 bv[4];
#pragma unroll
  for (int g = 0; g < 4; g++) bv[g] = ld8(rb1, (8 * g + 4 * h + fb) * 2, 0);
  const unsigned aaddr = lds + G1_W + (8 * h + q) * 192 + nf * 64 + blk * 32 + pp * 8;
  const unsigned baddr = lds + G1_X + md * 8192 + l * 16;
  f32x16 acc = {};
  bf16x8 fa[2][2], fb_[2][2];
  auto rd = [&](int buf, unsigned aa, unsigned ba, int g) {
#pragma unroll
    for (int k = 0; k < 2; k++) {
      fa[buf][k] = trd2(aa + (2 * g + k) * 3072, aa + (2 * g + k) * 3072 + 768);
      fb_[buf][k] = lds16(ba + (2 * g + k) * 1024);
    }
    SB0();
  };
  auto mm = [&](int buf) { acc = mfma(fa[buf][0], fb_[buf][0], acc); acc = mfma(fa[buf][1], fb_[buf][1], acc); SB0(); };
  bar();
  STAMP(3);
  rd(0, aaddr, baddr, 0);
  unsigned xs = 0;
  for (int s = 0; s < 16; s++) {
    const unsigned aa = aaddr + (s & 3) * G1_WS, ba = baddr + xs;
    xs = xs == 2 * G1_XS ? 0 : xs + G1_XS;
    rd(1, aa, ba, 1); mm(0);
    rd(0, aa, ba, 2); mm(1);
    rd(1, aa, ba, 3); mm(0);
    bar();
    if (s < 15) rd(0, aaddr + ((s + 1) & 3) * G1_WS, baddr + xs, 0);
    mm(1);
  }
  STAMP(4);
  if (fb < GT * 16) {
#pragma unroll
    for (int g = 0; g < 4; g++) {
      float bs[4] = {blo(bv[g][0]), bhi(bv[g][0]), blo(bv[g][1]), bhi(bv[g][1])};
#pragma unroll
      for (int jj = 0; jj < 4; jj++) {
        float u = acc[4 * g + jj] + bs[jj];
        float e = __builtin_amdgcn_exp2f(-2.302208198f * u * (1.0f + 0.044715f * u * u));
        acc[4 * g + jj] = u * __builtin_amdgcn_rcpf(1.0f + e);
      }
    }
    const unsigned so = WS_G + ((dt * 2 + md) * GT + ft * 6 + nf * 2) * 1024 + l * 16;
    st16<SC1>(pack8u(acc, 0), rw, so);
    st16<SC1>(pack8u(acc, 1), rw, so + 1024);
  }
  drain();
  if (lds_arrive(lds + L_CTL + 8, 6)) {
    if (l == 0) put_flag(rw, WS_F3 + (dt * 16 + ft) * 128, tag);
  }
  STAMP(5);
}

DI float max3(float a, float b, float c) { float r; asm("v_max3_f32 %0, %1, %2, %3" : "=v"(r) : "v"(a), "v"(b), "v"(c)); return r; }
DI void softmax16(f32x16& p, float sc) {
  asm volatile("s_nop 11" : "+v"(p));
  float m0 = max3(p[0], p[1], p[2]), m1 = max3(p[3], p[4], p[5]), m2 = max3(p[6], p[7], p[8]), m3 = max3(p[9], p[10], p[11]), m4 = max3(p[12], p[13], p[14]);
  float mx = max3(max3(m0, m1, p[15]), max3(m2, m3, m4), m0) * sc;
#pragma unroll
  for (int i = 0; i < 16; i++) p[i] = __builtin_amdgcn_exp2f(p[i] * sc - mx);
  float s = (((p[0] + p[2]) + (p[4] + p[6])) + ((p[8] + p[10]) + (p[12] + p[14]))) + (((p[1] + p[3]) + (p[5] + p[7])) + ((p[9] + p[11]) + (p[13] + p[15])));
  s = __builtin_amdgcn_rcpf(s);
#pragma unroll
  for (int i = 0; i < 16; i++) p[i] *= s;
}

DI void g2_loader(const Params& p, unsigned lds, int rt, int hg, int rot, int j) {
  const int l = threadIdx.x & 63;
  rsrc_t rx = mk(p.x, S * D * 2), rq = mk(p.wqkv, D * 3 * D * 2), rp = mk(p.wphi, 32 * 256 * 2);
  unsigned xv[2];
#pragma unroll
  for (int i = 0; i < 2; i++) xv[i] = (j * 32 + i * 8 + (l >> 3)) * 1024 + (((l & 7) ^ ((4 * i + (l >> 4)) & 7)) * 16);
  const unsigned wv = (j * 16 + (l >> 3)) * 3072 + (((l & 7) ^ (((l >> 4) & 1) << 2)) * 16);
  auto issue = [&](int s, int slot) {
    const unsigned sidx = (s + rot) & 7, base = lds + slot * G2_SLOT;
    const unsigned xo = rt * (128 * 1024) + sidx * 128, wo = sidx * (64 * 3072) + hg * 128;
#pragma unroll
    for (int i = 0; i < 2; i++) {
      dma16(rx, xv[0], xo + i * 16384, base + G2_XO + j * 4096 + i * 2048);
      dma16(rx, xv[1], xo + i * 16384, base + G2_XO + j * 4096 + i * 2048 + 1024);
#pragma unroll
      for (int c = 0; c < 3; c++) dma16(rq, wv, wo + i * (8 * 3072) + c * 1024, base + G2_WO + c * 8192 + j * 2048 + i * 1024);
    }
  };
  issue(0, 0);
  issue(1, 1);
  wvb<10>();
  int slot = 2;
  for (int s = 0; s < 6; s++) {
    issue(s + 2, slot);
    slot = slot == 2 ? 0 : slot + 1;
    wvb<10>();
  }
  if (j < 2) dma16(rp, l * 16 + j * 1024, hg * 2048, lds + G2_PHI + j * 1024);
  wvb<0>();
  bar(); bar(); bar();
}

DI void g2_compute(const Params& p, unsigned lds, int rt, int hg, int w, unsigned tag) {
  const int t = threadIdx.x, l = t & 63;
  rsrc_t rw = mk(p.ws, WS_BYTES);
  if (t < 16) *(LAS unsigned*)(lds + G2_ONES + t * 4) = 0x3f803f80u;
  const int h = l >> 5, blk = (l >> 4) & 1, q = (l & 15) >> 2, pp = l & 3;
  const int mq = w >> 1, hp = w & 1;
  const unsigned xrow = mq * 32 + (l & 31);
  unsigned xa[4], wa_[4];
#pragma unroll
  for (int k = 0; k < 4; k++) {
    xa[k] = (lds + G2_XO + xrow * 128 + ((h ^ ((xrow >> 1) & 7)) * 16)) ^ ((k ^ mq) << 5);
    wa_[k] = lds + G2_WO + (8 * h + q) * 128 + (((hp * 4 + blk * 2 + (pp >> 1)) ^ (((q >> 1) & 1) << 2)) * 16) + (pp & 1) * 8 + ((k ^ mq) << 11);
  }
  f32x16 acc[3] = {};
  float cs[3] = {0.f, 0.f, 0.f};
  bf16x8 fx[2], fw[2][3];
  auto rd = [&](int buf, int k, unsigned sb) {
    fx[buf] = lds16(xa[k] + sb);
    const unsigned wb = wa_[k] + sb;
#pragma unroll
    for (int i = 0; i < 3; i++) fw[buf][i] = trd2(wb + i * 8192, wb + i * 8192 + 512);
    SB0();
  };
  auto mm = [&](int buf, bool c) {
#pragma unroll
    for (int i = 0; i < 3; i++) {
      acc[i] = mfma(fw[buf][i], fx[buf], acc[i]);
      if (c) {
        u32x4 u = __builtin_bit_cast(u32x4, fw[buf][i]);
#pragma unroll
        for (int jj = 0; jj < 4; jj++) cs[i] = dot2(u[jj], 0x3f803f80u, cs[i]);
      }
    }
    SB0();
  };
  bar();
  STAMP(2);
  rd(0, 0, 0);
  unsigned sb = 0;
  for (int s = 0; s < 8; s++) {
    rd(1, 1, sb); mm(0, true);
    rd(0, 2, sb); mm(1, false);
    rd(1, 3, sb); mm(0, false);
    sb = sb == 2 * G2_SLOT ? 0 : sb + G2_SLOT;
    if (s < 7) { bar(); rd(0, 0, sb); }
    mm(1, false);
  }
  STAMP(3);
  u32x4 sg = ld16<SC1>(rw, WS_SB + (rt * 128 + xrow) * 16, 0);
#pragma unroll
  for (int i = 0; i < 3; i++) *(LAS float*)(lds + G2_CSP + ((hp * 3 + i) * 32 + (l & 31)) * 32 + (mq * 2 + h) * 4) = cs[i];
  bar();
  if (t < 192) {
    f32x4 a = *(LAS f32x4*)(lds + G2_CSP + t * 32), b = *(LAS f32x4*)(lds + G2_CSP + t * 32 + 16);
    *(LAS float*)(lds + G2_CS + t * 4) = ((a[0] + a[1]) + (a[2] + a[3])) + ((b[0] + b[1]) + (b[2] + b[3]));
  }
  bf16x8 wf[2];
#pragma unroll
  for (int e = 0; e < 2; e++) {
    unsigned a = lds + G2_PHI + (hp * 2 + e) * 512 + (4 * h + q) * 32 + (2 * blk + (pp >> 1)) * 8;
    u32x4 u = __builtin_bit_cast(u32x4, trd2(a, a + 8 * 32));
    unsigned sgn = (q & 1) ? 0x80008000u : 0u;
#pragma unroll
    for (int jj = 0; jj < 4; jj++) u[jj] ^= sgn;
    wf[e] = __builtin_bit_cast(bf16x8, u);
  }
#pragma nounroll
  for (int sp = 0; sp < SPIN_CAP && !__all(sg[1] == tag && sg[3] == tag); sp++) {
    asm volatile("" ::: "memory");
    sg = ld16<SC1>(rw, WS_SB + (rt * 128 + xrow) * 16, 0);
  }
  bar();
  const float r = asf(sg[0]), m = asf(sg[2]);
  {
    f32x4 c[12];
#pragma unroll
    for (int i = 0; i < 12; i++) c[i] = *(LAS f32x4*)(lds + G2_CS + ((hp * 3) * 32 + 8 * i + 4 * h) * 4);
#pragma unroll
    for (int i = 0; i < 3; i++)
#pragma unroll
      for (int g = 0; g < 4; g++)
#pragma unroll
        for (int jj = 0; jj < 4; jj++) {
          float v = acc[i][4 * g + jj] - m * c[i * 4 + g][jj];
          acc[i][4 * g + jj] = i == 2 ? v * r : v;
        }
  }
  STAMP(4);
  const float sc = r * 1.4426950408889634f;
  const unsigned fpk = ((xrow & 3) << 3) | ((xrow >> 1) & 7), fv = ((xrow << 1) | ((xrow >> 3) & 1)) & 15;
  const f32x16 z16 = {};
  f32x16 pk0 = mfma(wf[0], pack8(acc[1], 0), z16), pk1 = mfma(wf[1], pack8(acc[1], 1), z16);
#pragma unroll
  for (int g = 0; g < 4; g++) {
    u32x2 u = {pk(acc[2][4 * g], acc[2][4 * g + 1]), pk(acc[2][4 * g + 2], acc[2][4 * g + 3])};
    *(LAS u32x2*)(lds + G2_V + xrow * 128 + (((hp * 8 + 2 * g + h) ^ fv) * 8)) = u;
  }
  softmax16(pk0, sc);
  softmax16(pk1, sc);
#pragma unroll
  for (int g = 0; g < 4; g++) {
    u32x2 u = {pk(pk0[4 * g], pk0[4 * g + 1]), pk(pk0[4 * g + 2], pk0[4 * g + 3])};
    *(LAS u32x2*)(lds + G2_PK + xrow * 256 + ((((hp * 2) * 8 + 2 * g + h) ^ fpk) * 8)) = u;
    u32x2 u2 = {pk(pk1[4 * g], pk1[4 * g + 1]), pk(pk1[4 * g + 2], pk1[4 * g + 3])};
    *(LAS u32x2*)(lds + G2_PK + xrow * 256 + ((((hp * 2 + 1) * 8 + 2 * g + h) ^ fpk) * 8)) = u2;
  }
  f32x16 pq0 = mfma(wf[0], pack8(acc[0], 0), z16), pq1 = mfma(wf[1], pack8(acc[0], 1), z16);
  bar();
  STAMP(5);
  if (w < 4) {
    unsigned aa[2], ba[2], bs = blk ? 0 : 2048;
#pragma unroll
    for (int tt = 0; tt < 2; tt++) {
      unsigned sr = 8 * h + 4 * tt + q;
      unsigned f1 = ((sr & 3) << 3) | ((sr >> 1) & 7), f2 = ((sr << 1) | ((sr >> 3) & 1)) & 15;
      aa[tt] = lds + G2_PK + sr * 256 + (((w * 8 + blk * 4 + pp) ^ f1) * 8);
      ba[tt] = blk ? lds + G2_ONES + pp * 8 : lds + G2_V + sr * 128 + (((w * 4 + pp) ^ f2) * 8);
    }
    bf16x8 ka[8], kb[8];
#pragma unroll
    for (int ks = 0; ks < 8; ks++) {
      ka[ks] = trd2(aa[0] + ks * 4096, aa[1] + ks * 4096);
      kb[ks] = trd2(ba[0] + ks * bs, ba[1] + ks * bs);
    }
    SB0();
    f32x16 kv = {};
#pragma unroll
    for (int ks = 0; ks < 8; ks++) kv = mfma(ka[ks], kb[ks], kv);
    const int head = hg * 4 + w, c = l & 31;
    const unsigned off = c < 16 ? WS_KVP + (rt * 32 + head) * 1024 + (h * 32 + c) * 16 : c == 16 ? WS_ZP + head * 1024 + (h * 32 + rt) * 16 : OOB;
    st16<SC1>(pack8u(kv, 0), rw, off);
    st16<SC1>(pack8u(kv, 1), rw, off + 256);
    drain();
    if (lds_arrive(lds + L_CTL, 4)) {
      if (l == 0) put_flag(rw, WS_F2 + (hg * 16 + rt) * 128, tag);
    }
  }
  STAMP(6);
  softmax16(pq0, sc);
  softmax16(pq1, sc);
  bf16x8 phq[2][2] = {{pack8(pq0, 0), pack8(pq0, 1)}, {pack8(pq1, 0), pack8(pq1, 1)}};
  STAMP(7);
  bar();
  STAMP(8);
  {
    const int hh = w & 3, half = w >> 2, head = hg * 4 + hh;
    u32x4 v[8];
#pragma unroll
    for (int i = 0; i < 8; i++) v[i] = ld16<SC1>(rw, l * 16 + ((half * 8 + i) * 32 + head) * 1024, WS_KVP);
    u32x4 zv = ld16<SC1>(rw, l * 16 + head * 1024, half == 0 ? WS_ZP : OOB);
    float s[8] = {};
    unsigned sel_lo = 0x00003f80u;
    asm("" : "+s"(sel_lo));
#pragma unroll
    for (int i = 0; i < 8; i++)
#pragma unroll
      for (int jj = 0; jj < 4; jj++) { s[2 * jj] = dot2(v[i][jj], sel_lo, s[2 * jj]); s[2 * jj + 1] = dot2(v[i][jj], 0x3f800000u, s[2 * jj + 1]); }
    u32x4 o = {pk(s[0], s[1]), pk(s[2], s[3]), pk(s[4], s[5]), pk(s[6], s[7])};
    const unsigned ta = lds + G2_T + (half * 4 + hh) * 2048 + ((l >> 4) & 1) * 1024 + ((l >> 5) * 32 + (l & 15)) * 16;
    *(LAS u32x4*)ta = o;
    *(LAS u32x4*)(ta + 256) = zv;
  }
  bar();
  bf16x8 ta_[2][4];
#pragma unroll
  for (int e = 0; e < 2; e++)
#pragma unroll
    for (int c = 0; c < 4; c++) ta_[e][c] = lds16(lds + G2_T + ((c >> 1) * 4 + hp * 2 + e) * 2048 + (c & 1) * 1024 + l * 16);
  SB0();
  bar();
  f32x16 a[2] = {};
#pragma unroll
  for (int c = 0; c < 4; c++)
#pragma unroll
    for (int e = 0; e < 2; e++) a[e] = mfma(ta_[e][c], phq[e][c & 1], a[e]);
#pragma unroll
  for (int e = 0; e < 2; e++) {
    float den = ((a[e][8] + a[e][9]) + (a[e][10] + a[e][11])) + ((a[e][12] + a[e][13]) + (a[e][14] + a[e][15]));
    den = __builtin_amdgcn_rcpf(half_sum(den) + 1e-6f);
#pragma unroll
    for (int i = 0; i < 8; i++) a[e][i] *= den;
    st16<SC1>(pack8u(a[e], 0), rw, WS_O + l * 16 + ((rt * 4 + mq) * 32 + hg * 4 + hp * 2 + e) * 1024);
  }
  drain();
  if (lds_arrive(lds + L_CTL + 4, 8)) {
    if (l == 0) put_flag(rw, WS_F4 + (rt * 8 + hg) * 128, tag);
  }
  STAMP(9);
}

DI int p2_rot(int st, int dt) { return (((st & 7) * 11) >> 3) + 3 * (dt & 3); }
struct P2T {
  rsrc_t rw2, rwo;
  unsigned tv2, tvo, base;
  int rot, st, dt;
  DI void init(const Params& p, unsigned lds, int st_, int dt_, int j) {
    const int l = threadIdx.x & 63;
    st = st_; dt = dt_;
    rw2 = mk(p.w2, F * S * 2); rwo = mk(p.wo, D * D * 2);
    const unsigned trow = j * 64 + (l >> 3), tch = ((l & 7) ^ (((l >> 4) & 1) << 2)) * 16;
    tv2 = trow * 4096 + tch; tvo = trow * 1024 + tch;
    base = lds + P2_T + j * 8192;
    rot = p2_rot(st, dt);
  }
  DI void issue(int s, int slot) {
    const unsigned b = base + slot * P2_S;
    if (s < 11) {
      const unsigned to = ((s + rot) % 11) * (128 * 4096) + st * 128;
#pragma unroll
      for (int i = 0; i < 8; i++) dma16(rw2, tv2, to + i * (8 * 4096), b + i * 1024);
    } else {
      const unsigned to = ((s + rot) & 3) * (128 * 1024) + dt * 128;
#pragma unroll
      for (int i = 0; i < 8; i++) dma16(rwo, tvo, to + i * (8 * 1024), b + i * 1024);
    }
  }
  DI void prologue() {
#pragma nounroll
    for (int s = 0; s < 6; s++) issue(s, s);
  }
  DI void run() {
    wvb<40>();
    wvb<32>();
    int slot = 0;
    for (int s = 1; s < 10; s++) {
      issue(s + 5, slot);
      slot = slot == 5 ? 0 : slot + 1;
      wvb<32>();
    }
    wvb<24>(); wvb<16>(); wvb<8>(); wvb<0>();
    bar();
  }
};
DI void p2_lloader(const Params& p, unsigned lds, int st, int dt, int j, unsigned tag) {
  const int l = threadIdx.x & 63;
  rsrc_t rw = mk(p.ws, WS_BYTES);
  const int rot = p2_rot(st, dt);
  auto issue = [&](int s, int slot) {
    const unsigned b = lds + P2_L + slot * P2_S + j * 8192;
    const unsigned lo = s < 11 ? WS_G + ((dt * 2 + j) * GT + ((s + rot) % 11) * 8) * 1024 : WS_O + ((st * 2 + j) * 32 + ((s + rot) & 3) * 8) * 1024;
    dma16x4_sc1(rw, l * 16, lo, b);
    dma16x4_sc1(rw, l * 16, lo + 4096, b + 4096);
  };
  poll_flags(rw, (l & 31) < 15 ? WS_F3 + (dt * 16 + (l & 31)) * 128 : WS_F4 + ((st >> 1) * 8 + (l & 7)) * 128, tag);
  STAMPW(10, j == 0);
  issue(0, 0); issue(1, 1);
  wvb<8>();
  int slot = 2;
  for (int s = 0; s < 13; s++) {
    issue(s + 2, slot);
    slot = slot == 2 ? 0 : slot + 1;
    wvb<8>();
  }
  wvb<0>();
  bar();
}
DI void p2_compute(const Params& p, unsigned lds, int st, int dt, int w) {
  const int t = threadIdx.x, l = t & 63;
  rsrc_t rx = mk(p.x, S * D * 2), rb2 = mk(p.b2, S * 2), ro = mk(p.out, S * D * 2);
  const unsigned ov = (t >> 3) * 1024 + (t & 7) * 16 + st * 65536 + dt * 128;
  u32x4 xres = ld16(rx, ov, 0);
  unsigned short b2v = __builtin_amdgcn_raw_buffer_load_b16(rb2, (t >> 3) * 2, st * 128, 0);
  const int h = l >> 5, blk = (l >> 4) & 1, q = (l & 15) >> 2, pp = l & 3;
  const int kh = w >> 2, ms = (w >> 1) & 1, nd = w & 1;
  const unsigned tb = lds + P2_T + kh * 8192 + (4 * h + q) * 128 + (pp & 1) * 8;
  const unsigned sw = ((q >> 1) & 1) << 2;
  const unsigned tu = tb + (((ms * 4 + blk * 2 + (pp >> 1)) ^ sw) * 16), to = tb + (((nd * 4 + blk * 2 + (pp >> 1)) ^ sw) * 16);
  const unsigned lu = lds + P2_L + nd * 8192 + kh * 4096 + l * 16, lo = lds + P2_L + ms * 8192 + kh * 4096 + l * 16;
  f32x16 acc = {};
  bf16x8 ft_[2][2], fl[2][2];
  auto rd = [&](int buf, unsigned ta, unsigned la, int g) {
#pragma unroll
    for (int k = 0; k < 2; k++) {
      ft_[buf][k] = trd2(ta + (2 * g + k) * 2048, ta + (2 * g + k) * 2048 + 1024);
      fl[buf][k] = lds16(la + (2 * g + k) * 1024);
    }
    SB0();
  };
  auto mmu = [&](int buf) { acc = mfma(fl[buf][0], ft_[buf][0], acc); acc = mfma(fl[buf][1], ft_[buf][1], acc); SB0(); };
  auto mmo = [&](int buf) { acc = mfma(ft_[buf][0], fl[buf][0], acc); acc = mfma(ft_[buf][1], fl[buf][1], acc); SB0(); };
  bar();
  STAMP(11);
  rd(0, tu, lu, 0);
  unsigned ts = 0, ls = 0;
  for (int s = 0; s < 11; s++) {
    rd(1, tu + ts, lu + ls, 1); mmu(0);
    ts = ts == 5 * P2_S ? 0 : ts + P2_S;
    ls = ls == 2 * P2_S ? 0 : ls + P2_S;
    bar();
    rd(0, (s < 10 ? tu : to) + ts, (s < 10 ? lu : lo) + ls, 0);
    mmu(1);
  }
  for (int s = 11; s < 15; s++) {
    rd(1, to + ts, lo + ls, 1); mmo(0);
    ts = ts == 5 * P2_S ? 0 : ts + P2_S;
    ls = ls == 2 * P2_S ? 0 : ls + P2_S;
    if (s < 14) { bar(); rd(0, to + ts, lo + ls, 0); }
    mmo(1);
  }
  STAMP(12);
  const unsigned rb = lds + P2_T + 3 * P2_S;
#pragma unroll
  for (int g = 0; g < 4; g++) {
    f32x4 v = {acc[4 * g], acc[4 * g + 1], acc[4 * g + 2], acc[4 * g + 3]};
    *(LAS f32x4*)(rb + kh * 17408 + (ms * 32 + (l & 31)) * 272 + (nd * 32 + 8 * g + 4 * h) * 4) = v;
  }
  bar();
  {
    const unsigned ra = rb + (t >> 3) * 272 + (t & 7) * 32;
    f32x4 a0 = *(LAS f32x4*)ra, a1 = *(LAS f32x4*)(ra + 16), c0 = *(LAS f32x4*)(ra + 17408), c1 = *(LAS f32x4*)(ra + 17408 + 16);
    const float bb = blo(b2v);
    a0 += c0;
    a1 += c1;
    u32x4 o = {pk(a0[0] + bb + blo(xres[0]), a0[1] + bb + bhi(xres[0])), pk(a0[2] + bb + blo(xres[1]), a0[3] + bb + bhi(xres[1])),
               pk(a1[0] + bb + blo(xres[2]), a1[1] + bb + bhi(xres[2])), pk(a1[2] + bb + blo(xres[3]), a1[3] + bb + bhi(xres[3]))};
    st16(o, ro, ov);
  }
  STAMP(13);
}

extern __shared__ __attribute__((aligned(128))) char smem[];
DI unsigned lds_base() { return (unsigned)(size_t)(LAS char*)smem; }

__global__ __launch_bounds__(NT, 3) void k_main(Params p) {
  const int b = blockIdx.x, xc = b & 7, i = b >> 3, l = threadIdx.x & 63, w = __builtin_amdgcn_readfirstlane(threadIdx.x >> 6);
  const unsigned lds = lds_base();
  STAMP(0);
  rsrc_t rw = mk(p.ws, WS_BYTES);
  const int st = (xc >> 1) * 8 + (i >> 2), dt2 = (xc & 1) * 4 + (i & 3);
  const bool g1 = i < 16;
  const bool crew = g1 && (w < 6 || w >= 10);
  const int c = w < 6 ? w : w - 4, lb = xc * 16 + (i & 15);
  u32x4 v0, v1;
  if (crew) {
    rsrc_t rx = mk(p.x, S * D * 2);
    const unsigned o = (lb * 16 + c * 2 + (l >> 5)) * 1024 + (l & 31) * 16;
    v0 = ld16(rx, o, 0);
    v1 = ld16(rx, o + 512, 0);
  }
  unsigned seedv = __builtin_amdgcn_raw_buffer_load_b32(rw, WS_SEED, 0, SC1);
  if (threadIdx.x < 4) *(LAS unsigned*)(lds + L_CTL + threadIdx.x * 4) = 0u;
  bar();
  P2T pt;
  if (g1) {
    const int dt = (xc & 1) * 4 + (i & 3), ft = (xc >> 1) * 4 + (i >> 2);
    const int rot = (4 * (i & 3) + 5 * (i >> 2) + 2 * xc) & 15;
    if (!crew) {
      if (ft < 15) { g1_wloader(p, lds, ft, rot, w - 6); bar(); }
    } else {
      const unsigned tag = __builtin_amdgcn_readfirstlane(seedv) + 1u;
      ln_phase(p, lds, lb, c, tag, v0, v1);
      STAMP(1);
      if (ft < 15) {
        if (w < 6) g1_compute(p, lds, dt, ft, w, tag);
        else { g1_xloader(p, lds, dt, rot, w - 10, tag); bar(); }
      } else if (b == 6 + 8 * 12 && w == 0) {
        poll_ln(rw, tag);
        poll_flags(rw, WS_F2 + l * 128, tag);
        poll_flags(rw, WS_F2 + 8192 + l * 128, tag);
        if (l == 0) __builtin_amdgcn_raw_buffer_store_b32(tag, rw, WS_SEED, 0, SC1);
      }
    }
    if (ft >= 15) bar();
    if (w == 8 || w == 9) { pt.init(p, lds, st, dt2, w - 8); pt.prologue(); }
  } else {
    const int rt = (xc >> 1) * 4 + (i & 3), hg = (xc & 1) * 4 + ((i >> 2) & 3);
    if (w >= 8) {
      g2_loader(p, lds, rt, hg, ((i & 3) * 2 + ((i >> 2) & 3) * 3) & 7, w - 8);
      if (w < 10) { pt.init(p, lds, st, dt2, w - 8); pt.prologue(); }
      if (w == 11) poll_flags(rw, WS_F2 + (hg * 16 + (l & 15)) * 128, __builtin_amdgcn_readfirstlane(seedv) + 1u);
      bar(); bar(); bar();
    } else
      g2_compute(p, lds, rt, hg, w, __builtin_amdgcn_readfirstlane(seedv) + 1u);
  }
  if (w < 8) p2_compute(p, lds, st, dt2, w);
  else if (w < 10) pt.run();
  else p2_lloader(p, lds, st, dt2, w - 10, __builtin_amdgcn_readfirstlane(seedv) + 1u);
}
}

extern "C" void kernel_launch(void* const* d_in, const int* in_sizes, int n_in, void* d_out, int out_size, void* d_ws, size_t ws_size,
                              hipStream_t stream) {
  using namespace hk;
  Params p{d_in[0], d_in[1], d_in[2], d_in[3], d_in[5], d_in[6], d_in[7], d_in[8], d_out, d_ws};
  (void)hipFuncSetAttribute((const void*)k_main, hipFuncAttributeMaxDynamicSharedMemorySize, LDS_BYTES);
  k_main<<<256, NT, LDS_BYTES, stream>>>(p);
}
```

```cpp
#include <hip/hip_runtime.h>
#include <hip/hip_bf16.h>
#define FUSE_P2 1
#define MIXED 1
#define W_EARLY 0
#define SPIN_SLEEP 1
#define POLL_SLEEP 1
#define LPRIO 0
#define CPRIO 3
#define T_PACE 40
#define T_SLEEP 0
#define T_GATE 16
#define W_GATE 12
#define CODE_BYTES 30720

namespace hk {
#define DI __device__ __forceinline__
#define LAS __attribute__((address_space(3)))
typedef short bf16x8 __attribute__((ext_vector_type(8)));
typedef short s16x4 __attribute__((ext_vector_type(4)));
typedef float f32x16 __attribute__((ext_vector_type(16)));
typedef float f32x4 __attribute__((ext_vector_type(4)));
typedef float f32x2 __attribute__((ext_vector_type(2)));
typedef unsigned u32x4 __attribute__((ext_vector_type(4)));
typedef unsigned u32x2 __attribute__((ext_vector_type(2)));
typedef __bf16 bfx2 __attribute__((ext_vector_type(2)));
typedef __amdgpu_buffer_rsrc_t rsrc_t;

constexpr int S = 2048, D = 512, F = 1376;
constexpr int GT = 88;
constexpr int WS_SEED = 0;
constexpr int WS_F2 = 4096;
constexpr int WS_F3 = 20480;
constexpr int WS_F4 = 36864;
constexpr int WS_CS = 53248;
constexpr int WS_SB = 65536;
constexpr int WS_ZP = 98304;
constexpr int WS_KVP = 131072;
constexpr int WS_G = 1 << 20;
constexpr int WS_O = 3 << 20;
constexpr int WS_H = 5 << 20;
constexpr int WS_BYTES = 7 << 20;
constexpr int SC1 = 16;
constexpr int NT = 768;
constexpr unsigned OOB = 0x40000000u;
constexpr int SPIN_CAP = 1 << 14;

#define STAMP(i)
#define STAMPW(i, cond)
struct Params {
  const void *x, *wqkv, *wphi, *wo, *w1, *b1, *w2, *b2;
  void *out, *ws;
};

#define SB0() __builtin_amdgcn_sched_barrier(0)
DI rsrc_t mk(const void* p, int bytes) { return __builtin_amdgcn_make_buffer_rsrc((void*)p, 0, bytes, 0x00020000); }
DI void dma16(rsrc_t r, unsigned voff, unsigned soff, unsigned lds) {
  asm volatile("s_mov_b32 m0, %0\n\ts_nop 0\n\tbuffer_load_dwordx4 %1, %2, %3 offen lds" ::"s"(lds), "v"(voff), "s"(r), "s"(soff) : "memory");
}
DI void dma16_sc1(rsrc_t r, unsigned voff, unsigned soff, unsigned lds) {
  asm volatile("s_mov_b32 m0, %0\n\ts_nop 0\n\tbuffer_load_dwordx4 %1, %2, %3 offen sc1 lds" ::"s"(lds), "v"(voff), "s"(r), "s"(soff) : "memory");
}
DI void dma16x4_sc1(rsrc_t r, unsigned voff, unsigned soff, unsigned lds) {
  asm volatile("s_mov_b32 m0, %0\n\ts_nop 0\n\tbuffer_load_dwordx4 %1, %2, %3 offen sc1 lds\n\tbuffer_load_dwordx4 %1, %2, %3 offen offset:1024 sc1 lds\n\t"
               "buffer_load_dwordx4 %1, %2, %3 offen offset:2048 sc1 lds\n\tbuffer_load_dwordx4 %1, %2, %3 offen offset:3072 sc1 lds" ::"s"(lds), "v"(voff), "s"(r), "s"(soff) : "memory");
}
DI unsigned lds_tag(unsigned lds);
DI void lds_set(unsigned a) { *(volatile LAS unsigned*)a = 1u; }
template <int N> DI void wvb() { asm volatile("s_waitcnt vmcnt(%0) lgkmcnt(0)\n\ts_barrier" ::"i"(N) : "memory"); }
DI void bar() { asm volatile("s_waitcnt lgkmcnt(0)\n\ts_barrier" ::: "memory"); }
DI void drain() { asm volatile("s_waitcnt vmcnt(0)" ::: "memory"); }
template <int AUX = 0> DI u32x4 ld16(rsrc_t r, unsigned voff, unsigned soff) { return __builtin_amdgcn_raw_buffer_load_b128(r, voff, soff, AUX); }
template <int AUX = 0> DI u32x2 ld8(rsrc_t r, unsigned voff, unsigned soff) { return __builtin_amdgcn_raw_buffer_load_b64(r, voff, soff, AUX); }
template <int AUX = 0> DI void st16(u32x4 v, rsrc_t r, unsigned voff) { __builtin_amdgcn_raw_buffer_store_b128(v, r, voff, 0, AUX); }
DI s16x4 trd(unsigned a) { return __builtin_amdgcn_ds_read_tr16_b64_v4i16((LAS s16x4*)a); }
DI bf16x8 trd2(unsigned a0, unsigned a1) {
  s16x4 lo = trd(a0), hi = trd(a1);
  return __builtin_shufflevector(lo, hi, 0, 1, 2, 3, 4, 5, 6, 7);
}
DI bf16x8 lds16(unsigned a) { return *(LAS bf16x8*)a; }
DI f32x16 mfma(bf16x8 a, bf16x8 b, f32x16 c) { return
 __builtin_amdgcn_mfma_f32_32x32x16_bf16(a, b, c, 0, 0, 0); }
DI unsigned pk(float lo, float hi) {
  f32x2 v = {lo, hi};
  return __builtin_bit_cast(unsigned, __builtin_convertvector(v, bfx2));
}
DI float asf(unsigned u) { return __builtin_bit_cast(float, u); }
DI unsigned asu(float u) { return __builtin_bit_cast(unsigned, u); }
DI float blo(unsigned u) { return asf(u << 16); }
DI float bhi(unsigned u) { return asf(u & 0xffff0000u); }
DI u32x4 pack8u(const f32x16& x, int s) {
  u32x4 p = {pk(x[8 * s], x[8 * s + 1]), pk(x[8 * s + 2], x[8 * s + 3]), pk(x[8 * s + 4], x[8 * s + 5]), pk(x[8 * s + 6], x[8 * s + 7])};
  return p;
}
DI bf16x8 pack8(const f32x16& x, int s) { return __builtin_bit_cast(bf16x8, pack8u(x, s)); }
DI float dot2(unsigned a, unsigned b, float c) { return __builtin_amdgcn_fdot2_f32_bf16(__builtin_bit_cast(bfx2, a), __builtin_bit_cast(bfx2, b), c, false); }
DI float half_sum(float v) {
  unsigned u = asu(v);
  auto r = __builtin_amdgcn_permlane32_swap(u, u, false, false);
  return asf(r[0]) + asf(r[1]);
}
template <int C> DI float dpp_add(float v) { return v + __builtin_amdgcn_update_dpp(0.f, v, C, 0xf, 0xf, false); }
DI float half32_sum(float v) {
  v = dpp_add<0x128>(v);
  v = dpp_add<0x124>(v);
  v = dpp_add<0x122>(v);
  v = dpp_add<0x121>(v);
  unsigned u = asu(v);
  auto r = __builtin_amdgcn_permlane16_swap(u, u, false, false);
  return asf(r[0]) + asf(r[1]);
}
DI void put_flag(rsrc_t rw, unsigned off, unsigned tag) {
  u32x2 f = {tag, ~tag};
  __builtin_amdgcn_raw_buffer_store_b64(f, rw, off, 0, SC1);
}
DI void poll_flags(rsrc_t rw, unsigned off, unsigned tag) {
#pragma nounroll
  for (int sp = 0; sp < SPIN_CAP; sp++) {
    asm volatile("" ::: "memory");
    u32x2 f = ld8<SC1>(rw, off, 0);
    if (__all(f[0] == tag && f[1] == ~tag)) break;
    __builtin_amdgcn_s_sleep(1);
  }
  asm volatile("" ::: "memory");
}
DI bool lds_arrive(unsigned cnt, unsigned n) {
  asm volatile("s_waitcnt lgkmcnt(0)" ::: "memory");
  unsigned old = 0;
  if ((threadIdx.x & 63) == 0) old = __hip_atomic_fetch_add((LAS unsigned*)cnt, 1u, __ATOMIC_RELAXED, __HIP_MEMORY_SCOPE_WORKGROUP);
  old = __builtin_amdgcn_readfirstlane(old);
  asm volatile("" ::: "memory");
  return old == n - 1;
}
DI void lds_wait(unsigned cnt, unsigned n) {
  while (__hip_atomic_load((LAS unsigned*)cnt, __ATOMIC_RELAXED, __HIP_MEMORY_SCOPE_WORKGROUP) < n) __builtin_amdgcn_s_sleep(SPIN_SLEEP);
  asm volatile("" ::: "memory");
}

DI void prog_put(unsigned a, unsigned v) { asm volatile("" ::: "memory"); *(volatile LAS unsigned*)a = v; asm volatile("" ::: "memory"); }
DI void prog_wait(unsigned a, unsigned n) {
  unsigned seen;
  asm volatile(
      ".Lpw_loop%=:\n\tds_read_b32 %0, %1\n\ts_waitcnt lgkmcnt(0)\n\t"
      "v_cmp_le_u32 vcc, %2, %0\n\ts_cmp_eq_u64 vcc, exec\n\ts_cbranch_scc0 .Lpw_loop%="
      : "=&v"(seen) : "v"(a), "s"(n) : "vcc", "scc", "memory");
}
constexpr int L_CTL = 147456;
constexpr int L_PW = L_CTL + 320;
constexpr int L_MISC = L_CTL + 512;
constexpr int LDS_BYTES = L_MISC + 9216;
constexpr int G1_W = 0, G1_WS = 24576, G1_X = 4 * G1_WS, G1_XS = 16384;
constexpr int G2_SLOT = 40960, G2_XO = 0, G2_WO = 16384, G2_PHI = L_MISC, G2_ONES = G2_PHI + 2048, G2_CS = G2_ONES + 64, G2_ST = G2_CS + 768;
constexpr int G2_PK = 98304, G2_V = G2_PK + 32768, G2_T = G2_V;
constexpr int P2_T = 0, P2_L = 98304, P2_S = 16384;
static_assert(G2_ST + 1024 <= LDS_BYTES && LDS_BYTES <= 163840, "lds");

DI unsigned lds_tag(unsigned lds) { return __builtin_amdgcn_readfirstlane(*(volatile LAS unsigned*)(lds + L_CTL + 24)); }

DI void ln_phase(const Params& p, unsigned lds, int lb, int c, unsigned tag, u32x4 v0, u32x4 v1) {
  const int l = threadIdx.x & 63;
  rsrc_t rw = mk(p.ws, WS_BYTES);
  float s = 0.f, s1 = 0.f;
#pragma unroll
  for (int i = 0; i < 4; i++) { s = dot2(v0[i], 0x3f803f80u, s); s1 = dot2(v1[i], 0x3f803f80u, s1); }
  const float m = half32_sum(s + s1) * (1.0f / D);
  f32x2 e[8], se = {0.f, 0.f}, sq = {0.f, 0.f};
#pragma unroll
  for (int i = 0; i < 4; i++) {
    e[i] = f32x2{blo(v0[i]), bhi(v0[i])} - m;
    e[4 + i] = f32x2{blo(v1[i]), bhi(v1[i])} - m;
  }
#pragma unroll
  for (int i = 0; i < 8; i++) { se += e[i]; sq += e[i] * e[i]; }
  const float me = half32_sum(se[0] + se[1]) * (1.0f / D), var = half32_sum(sq[0] + sq[1]) * (1.0f / D);
  const float r = __builtin_amdgcn_rsqf(var + 1e-5f);
  const float m2 = me * r, v2 = fmaxf(var - me * me, 0.f) * r * r;
  const float r2 = __builtin_amdgcn_rsqf(v2 + 1e-5f);
  const float ca = r * r2, cb = -m2 * r2;
  u32x4 o0, o1;
#pragma unroll
  for (int i = 0; i < 4; i++) {
    f32x2 u = e[i] * ca + cb, u2 = e[4 + i] * ca + cb;
    o0[i] = pk(u[0], u[1]);
    o1[i] = pk(u2[0], u2[1]);
  }
  const int row = c * 2 + (l >> 5);
  const unsigned ho = WS_H + ((l & 31) >> 3) * (S * 128) + (lb * 16 + row) * 128 + (((l & 7) ^ ((c & 1) << 2)) * 16);
  st16<SC1>(o0, rw, ho);
  st16<SC1>(o1, rw, ho + 4 * (S * 128));
  if ((l & 31) == 0) {
    u32x4 g = {asu(r), tag, asu(m), tag};
    *(LAS u32x4*)(lds + L_CTL + 64 + row * 16) = g;
  }
  if (W_GATE == 0) lds_arrive(lds + L_CTL, 8);
  drain();
  if (lds_arrive(lds + L_CTL + 4, 8)) {
    if (l < 16) {
      u32x4 g = *(LAS u32x4*)(lds + L_CTL + 64 + l * 16);
      st16<SC1>(g, rw, WS_SB + lb * 256 + l * 16);
    }
    lds_set(lds + L_CTL + 12);
    STAMPW(8, true);
    STAMPW(15, true);
  }
}
DI void poll_ln(rsrc_t rw, unsigned tag) {
  const int l = threadIdx.x & 63;
  bool ok0 = false, ok1 = false;
#pragma nounroll
  for (int sp = 0; sp < SPIN_CAP; sp++) {
    asm volatile("" ::: "memory");
    u32x4 f0 = ld16<SC1>(rw, ok0 ? OOB : WS_SB + l * 256, 0), f1 = ld16<SC1>(rw, ok1 ? OOB : WS_SB + 16384 + l * 256, 0);
    ok0 = ok0 || (f0[1] == tag && f0[3] == tag);
    ok1 = ok1 || (f1[1] == tag && f1[3] == tag);
    if (__all(ok0 && ok1)) break;
    __builtin_amdgcn_s_sleep(POLL_SLEEP);
  }
  asm volatile("" ::: "memory");
}

DI int g1_stage(int s, int rot) { return (s + rot) & 15; }
DI int p2_rot(int st, int dt) { return (((st & 7) * 11) >> 3) + 6 * (dt & 1) + 3 * ((dt >> 1) & 1); }
struct TW {
  rsrc_t rw2;
  unsigned tv2, base;
  int rot, st;
  DI TW(const Params& p, unsigned lds, int st_, int dt, int j) {
    const int l = threadIdx.x & 63;
    rw2 = mk(p.w2, F * S * 2);
    tv2 = (j * 32 + (l >> 3)) * 4096 + ((l & 7) ^ (((l >> 4) & 1) << 2)) * 16;
    base = lds + j * 4096; rot = p2_rot(st_, dt); st = st_;
  }
  DI void operator()(int s, int slot) const {
    const unsigned to = ((s + rot) % 11) * (128 * 4096) + st * 128;
#pragma unroll
    for (int i = 0; i < 4; i++) dma16(rw2, tv2, to + i * (8 * 4096), base + slot * P2_S + i * 1024);
  }
};
template <int N> DI void wv() { asm volatile("s_waitcnt vmcnt(%0)" ::"i"(N) : "memory"); }
DI void g1_wloader(const Params& p, unsigned lds, int ft, int rot, int j, int st, int dt2) {
  const int l = threadIdx.x & 63;
  rsrc_t rw1 = mk(p.w1, S * F * 2);
  unsigned wvo[3];
#pragma unroll
  for (int i = 0; i < 3; i++) {
    unsigned u = i * 64 + l;
    unsigned row = u / 12, ch = u - row * 12;
    wvo[i] = (ft * 96 + ch * 8 >= F) ? OOB : (j * 32 + row) * (F * 2) + ch * 16;
  }
  const unsigned wl = lds + G1_W + j * 6144;
  auto issue = [&](int q) {
    const unsigned so = g1_stage(q, rot) * (128 * F * 2) + ft * 192, dst = wl + (q & 3) * G1_WS;
#pragma unroll
    for (int k = 0; k < 6; k++) dma16(rw1, wvo[k % 3], so + (k / 3) * (16 * F * 2), dst + k * 1024);
  };
#pragma nounroll
  for (int q = 0; q < 4; q++) {
    if (q == W_EARLY) lds_wait(lds + L_CTL + W_GATE, W_GATE ? 1 : 8);
    issue(q);
  }
  wvb<18>();
  wvb<12>();
#pragma nounroll
  for (int q = 1; q < 13; q++) {
    issue(q + 3);
    wvb<12>();
  }
  const TW tw(p, lds, st, dt2, j);
  tw(0, 0);
  wvb<10>();
  tw(1, 1); tw(2, 2); tw(4, 7);
  wvb<16>();
  tw(3, 3); tw(5, 8);
  bar();
  drain();
}
DI void g1_xloader(const Params& p, unsigned lds, int dt, int rot, int j, unsigned tag) {
  const int l = threadIdx.x & 63;
  rsrc_t rw = mk(p.ws, WS_BYTES);
  auto issue = [&](int n, int slot) {
    const unsigned so = WS_H + dt * (S * 128) + g1_stage(n, rot) * 16384 + j * 8192, xd = lds + G1_X + slot * G1_XS + j * 8192;
    dma16x4_sc1(rw, l * 16, so, xd);
    dma16x4_sc1(rw, l * 16, so + 4096, xd + 4096);
  };
  if (j == 0) { poll_ln(rw, tag); lds_set(lds + L_CTL + 16); } else lds_wait(lds + L_CTL + 16, 1);
  STAMPW(2, j == 0);
  issue(0, 0); issue(1, 1);
  wvb<8>();
  int slot = 2;
#pragma nounroll
  for (int n = 0; n < 14; n++) {
    issue(n + 2, slot);
    slot = slot == 2 ? 0 : slot + 1;
    wvb<8>();
  }
  wvb<0>();
}
DI void g1_compute(const Params& p, unsigned lds, int dt, int ft, int c, unsigned tag) {
  const int l = threadIdx.x & 63;
  rsrc_t rw = mk(p.ws, WS_BYTES), rb1 = mk(p.b1, F * 2);
  const int h = l >> 5, blk = (l >> 4) & 1, q = (l & 15) >> 2, pp = l & 3;
  const int nf = c % 3, kh = c / 3;
  const int fb = ft * 96 + nf * 32;
  const unsigned a0 = lds + G1_W + (kh * 64 + 8 * h + q) * 192 + nf * 64 + blk * 32 + pp * 8;
  const unsigned b0 = (lds + G1_X + (kh * 64 + 8 * h + q) * 128 + (((blk * 2 + (pp >> 1)) ^ (((q >> 1) & 1) << 2)) * 16) + (pp & 1) * 8) ^ (kh * 64);
  u32x2 bv[4];
#pragma unroll
  for (int g = 0; g < 4; g++) bv[g] = ld8(rb1, (8 * g + 4 * h + fb) * 2, 0);
  f32x16 acc[2] = {};
#pragma unroll
  for (int g = 0; g < 4; g++) { acc[0][4 * g] = blo(bv[g][0]); acc[0][4 * g + 1] = bhi(bv[g][0]); acc[0][4 * g + 2] = blo(bv[g][1]); acc[0][4 * g + 3] = bhi(bv[g][1]); }
  bf16x8 fa[4], fx[4][2];
  unsigned aa = a0, ba = b0, bb = b0 ^ 64;
  auto rda = [&](int k) { fa[k] = trd2(aa + k * 3072, aa + k * 3072 + 768); fx[k][0] = trd2(ba + k * 2048, ba + k * 2048 + 512); };
  auto rdb = [&](int k) { fx[k][1] = trd2(bb + k * 2048, bb + k * 2048 + 512); };
  auto step = [&](int k, bool more) {
    acc[0] = mfma(fa[k], fx[k][0], acc[0]);
    SB0();
    if (more) rda(k ^ 2);
    SB0();
    acc[1] = mfma(fa[k], fx[k][1], acc[1]);
    SB0();
    if (more) rdb(k ^ 2);
    SB0();
  };
  unsigned exw = lds + 3 * G1_WS + l * 16 + c * 4096, exr = lds + 3 * G1_WS + l * 16 + (kh ? nf : nf + 3) * 4096;
  unsigned pwm = lds + L_PW + c * 4, pwo = lds + L_PW + (kh ? nf : nf + 3) * 4;
  unsigned so = fb < GT * 16 ? WS_G + ((dt * 2 + kh) * GT + ft * 6 + nf * 2) * 1024 + l * 16 : OOB;
  asm volatile("" : "+v"(exw), "+v"(exr), "+v"(pwm), "+v"(pwo), "+v"(so));
  bar();
  STAMP(3);
  rda(0); rdb(0); rda(1); rdb(1);
  unsigned xs = 0;
#pragma nounroll
  for (int n = 1; n < 16; n++) {
    step(0, true);
    step(1, true);
    xs = xs == 2 * G1_XS ? 0 : xs + G1_XS;
    aa = a0 + (n & 3) * G1_WS; ba = b0 + xs; bb = ba ^ 64;
    bar();
    step(2, true);
    step(3, true);
  }
  step(0, true);
  step(1, true);
  bar();
  acc[1] = mfma(fa[2], fx[2][1], acc[1]);
  acc[1] = mfma(fa[3], fx[3][1], acc[1]);
  acc[0] = mfma(fa[2], fx[2][0], acc[0]);
  acc[0] = mfma(fa[3], fx[3][0], acc[0]);
  SB0();
  STAMP(4);
#pragma unroll
  for (int g = 0; g < 4; g++) {
    f32x4 v = {acc[1][4 * g], acc[1][4 * g + 1], acc[1][4 * g + 2], acc[1][4 * g + 3]};
    *(LAS f32x4*)(exw + g * 1024) = v;
  }
  prog_put(pwm, 1);
  prog_wait(pwo, 1);
  u32x4 o[2];
#pragma unroll
  for (int g = 0; g < 4; g++) {
    const f32x4 v = *(LAS f32x4*)(exr + g * 1024);
#pragma unroll
    for (int jj = 0; jj < 2; jj++) {
      const f32x2 u = f32x2{acc[0][4 * g + 2 * jj], acc[0][4 * g + 2 * jj + 1]} + f32x2{v[2 * jj], v[2 * jj + 1]};
      const f32x2 z = u * (u * u * -0.1029432397f + -2.302208198f);
      const f32x2 d = f32x2{__builtin_amdgcn_exp2f(z[0]), __builtin_amdgcn_exp2f(z[1])} + 1.0f;
      const f32x2 r = u * f32x2{__builtin_amdgcn_rcpf(d[0]), __builtin_amdgcn_rcpf(d[1])};
      o[g >> 1][(g & 1) * 2 + jj] = pk(r[0], r[1]);
    }
  }
  st16<SC1>(o[0], rw, so);
  st16<SC1>(o[1], rw, so + 1024);
}

DI float max3(float a, float b, float c) { float r; asm("v_max3_f32 %0, %1, %2, %3" : "=v"(r) : "v"(a), "v"(b), "v"(c)); return r; }
DI void softmax16(f32x16& p, float sc) {
  asm volatile("s_nop 11" : "+v"(p));
  float m0 = max3(p[0], p[1], p[2]), m1 = max3(p[3], p[4], p[5]), m2 = max3(p[6], p[7], p[8]), m3 = max3(p[9], p[10], p[11]), m4 = max3(p[12], p[13], p[14]);
  float mx = max3(max3(m0, m1, p[15]), max3(m2, m3, m4), m0) * sc;
#pragma unroll
  for (int i = 0; i < 16; i++) p[i] = __builtin_amdgcn_exp2f(p[i] * sc - mx);
  float s = (((p[0] + p[2]) + (p[4] + p[6])) + ((p[8] + p[10]) + (p[12] + p[14]))) + (((p[1] + p[3]) + (p[5] + p[7])) + ((p[9] + p[11]) + (p[13] + p[15])));
  s = __builtin_amdgcn_rcpf(s);
#pragma unroll
  for (int i = 0; i < 16; i++) p[i] *= s;
}

DI void g2_loader(const Params& p, unsigned lds, int rt, int hg, int rot, int j) {
  const int l = threadIdx.x & 63;
  rsrc_t rx = mk(p.x, S * D * 2), rq = mk(p.wqkv, D * 3 * D * 2), rp = mk(p.wphi, 32 * 256 * 2);
  unsigned xv[2];
#pragma unroll
  for (int i = 0; i < 2; i++) xv[i] = (j * 32 + i * 8 + (l >> 3)) * 1024 + (((l & 7) ^ ((4 * i + (l >> 4)) & 7)) * 16);
  const unsigned wv = (j * 16 + (l >> 3)) * 3072 + (((l & 7) ^ (((l >> 4) & 1) << 2)) * 16);
  auto issue = [&](int s, int slot) {
    const unsigned sidx = (s + rot) & 7, base = lds + slot * G2_SLOT;
    const unsigned xo = rt * (128 * 1024) + sidx * 128, wo = sidx * (64 * 3072) + hg * 128;
#pragma unroll
    for (int i = 0; i < 2; i++) {
      dma16(rx, xv[0], xo + i * 16384, base + G2_XO + j * 4096 + i * 2048);
      dma16(rx, xv[1], xo + i * 16384, base + G2_XO + j * 4096 + i * 2048 + 1024);
#pragma unroll
      for (int c = 0; c < 3; c++) dma16(rq, wv, wo + i * (8 * 3072) + c * 1024, base + G2_WO + c * 8192 + j * 2048 + i * 1024);
    }
  };
  issue(0, 0);
  issue(1, 1);
  wvb<10>();
  int slot = 2;
  for (int s = 0; s < 6; s++) {
    issue(s + 2, slot);
    slot = slot == 2 ? 0 : slot + 1;
    wvb<10>();
  }
  if (j < 2) dma16(rp, l * 16 + j * 1024, hg * 2048, lds + G2_PHI + j * 1024);
  {
    rsrc_t rw = mk(p.ws, WS_BYTES);
    const unsigned tag = lds_tag(lds);
    const unsigned off = j < 2 ? WS_SB + (rt * 128 + j * 64 + l) * 16 : WS_CS + hg * 1536 + (j - 2) * 768 + (l < 48 ? l : 0) * 16;
    u32x4 g;
#pragma nounroll
    for (int sp = 0; sp < SPIN_CAP; sp++) {
      asm volatile("" ::: "memory");
      g = ld16<SC1>(rw, off, 0);
      if (__all(g[1] == tag && g[3] == tag)) break;
    }
    u32x2 o = {g[0], g[2]};
    if (j < 2 || l < 48) *(LAS u32x2*)(lds + (j < 2 ? G2_ST + j * 512 : G2_CS + (j - 2) * 384) + l * 8) = o;
  }
  wvb<0>();
  bar();
}
DI void cs_phase(const Params& p, unsigned lds, int cg, int j, int pass) {
  const int l = threadIdx.x & 63;
  rsrc_t rq = mk(p.wqkv, D * 3 * D * 2), rw = mk(p.ws, WS_BYTES);
  float s[8] = {};
  unsigned sel_lo = 0x00003f80u;
  asm("" : "+s"(sel_lo));
  const unsigned vo = (j * 128 + (l >> 3)) * 3072 + (l & 7) * 16 + cg * 128;
  u32x4 v[16];
#pragma unroll
  for (int u = 0; u < 16; u++) v[u] = ld16(rq, vo, u * (8 * 3072));
  SB0();
#pragma unroll
  for (int u = 0; u < 16; u++)
#pragma unroll
    for (int jj = 0; jj < 4; jj++) { s[2 * jj] = dot2(v[u][jj], sel_lo, s[2 * jj]); s[2 * jj + 1] = dot2(v[u][jj], 0x3f800000u, s[2 * jj + 1]); }
  const unsigned sc = pass ? lds : lds + L_MISC;
  const unsigned pa = sc + (j * 8 + (l >> 3)) * 272 + (l & 7) * 32;
  f32x4 a = {s[0], s[1], s[2], s[3]}, b = {s[4], s[5], s[6], s[7]};
  *(LAS f32x4*)pa = a;
  *(LAS f32x4*)(pa + 16) = b;
  if (lds_arrive(lds + L_CTL + 28 + pass * 4, 4)) {
    float t = 0.f;
#pragma unroll
    for (int r = 0; r < 32; r++) t += *(LAS float*)(sc + r * 272 + l * 4);
    lds_wait(lds + L_CTL + 52, 1);
    u32x2 g = {asu(t), lds_tag(lds)};
    __builtin_amdgcn_raw_buffer_store_b64(g, rw, WS_CS + (cg & 7) * 1536 + (((l >> 5) * 3 + (cg >> 3)) * 32 + (l & 31)) * 8, 0, SC1);
  }
}

DI void g2_compute(const Params& p, unsigned lds, int rt, int hg, int w) {
  const int t = threadIdx.x, l = t & 63;
  rsrc_t rw = mk(p.ws, WS_BYTES);
  if (t < 16) *(LAS unsigned*)(lds + G2_ONES + t * 4) = 0x3f803f80u;
  const int h = l >> 5, blk = (l >> 4) & 1, q = (l & 15) >> 2, pp = l & 3;
  const int mq = w >> 1, hp = w & 1;
  const unsigned xrow = mq * 32 + (l & 31);
  unsigned xa[4];
#pragma unroll
  for (int k = 0; k < 4; k++) xa[k] = (lds + G2_XO + xrow * 128 + ((h ^ ((xrow >> 1) & 7)) * 16)) ^ (k << 5);
  const unsigned wa_ = lds + G2_WO + (8 * h + q) * 128 + (((hp * 4 + blk * 2 + (pp >> 1)) ^ (((q >> 1) & 1) << 2)) * 16) + (pp & 1) * 8;
  f32x16 acc[3] = {};
  bf16x8 fx[4], fw[4][3];
  unsigned sb = 0;
  auto rd = [&](int k, int i) {
    if (i == 0) fx[k] = lds16(xa[k] + sb);
    const unsigned wb = wa_ + sb + k * 2048 + i * 8192;
    fw[k][i] = trd2(wb, wb + 512);
  };
  auto step = [&](int k, bool more) {
#pragma unroll
    for (int i = 0; i < 3; i++) {
      acc[i] = mfma(fw[k][i], fx[k], acc[i]);
      SB0();
      if (more) rd(k ^ 2, i);
      SB0();
    }
  };
  bar();
  STAMP(2);
#pragma unroll
  for (int i = 0; i < 6; i++) rd(i / 3, i % 3);
#pragma nounroll
  for (int s = 1; s < 8; s++) {
    step(0, true);
    step(1, true);
    sb = sb == 2 * G2_SLOT ? 0 : sb + G2_SLOT;
    bar();
    step(2, true);
    step(3, true);
  }
  step(0, true);
  step(1, true);
  bar();
  step(2, false);
  f32x4 c[12];
#pragma unroll
  for (int i = 0; i < 12; i++) c[i] = *(LAS f32x4*)(lds + G2_CS + ((hp * 3) * 32 + 8 * i + 4 * h) * 4);
  const f32x2 rm = *(LAS f32x2*)(lds + G2_ST + xrow * 8);
  bf16x8 wf[2];
#pragma unroll
  for (int e = 0; e < 2; e++) {
    unsigned a = lds + G2_PHI + (hp * 2 + e) * 512 + (4 * h + q) * 32 + (2 * blk + (pp >> 1)) * 8;
    wf[e] = trd2(a, a + 8 * 32);
  }
  SB0();
  step(3, false);
  STAMP(3);
#pragma unroll
  for (int e = 0; e < 2; e++) {
    u32x4 u = __builtin_bit_cast(u32x4, wf[e]);
    unsigned sgn = (q & 1) ? 0x80008000u : 0u;
#pragma unroll
    for (int jj = 0; jj < 4; jj++) u[jj] ^= sgn;
    wf[e] = __builtin_bit_cast(bf16x8, u);
  }
  const float r = rm[0], m = rm[1];
#pragma unroll
  for (int i = 0; i < 3; i++)
#pragma unroll
    for (int g = 0; g < 4; g++)
#pragma unroll
      for (int jj = 0; jj < 4; jj++) {
        float v = acc[i][4 * g + jj] - m * c[i * 4 + g][jj];
        acc[i][4 * g + jj] = i == 2 ? v * r : v;
      }
  STAMP(4);
  const float sc = r * 1.4426950408889634f;
  const unsigned fpk = ((xrow & 3) << 3) | ((xrow >> 1) & 7), fv = ((xrow << 1) | ((xrow >> 3) & 1)) & 15;
  const f32x16 z16 = {};
  f32x16 pk0 = mfma(wf[0], pack8(acc[1], 0), z16), pk1 = mfma(wf[1], pack8(acc[1], 1), z16);
#pragma unroll
  for (int g = 0; g < 4; g++) {
    u32x2 u = {pk(acc[2][4 * g], acc[2][4 * g + 1]), pk(acc[2][4 * g + 2], acc[2][4 * g + 3])};
    *(LAS u32x2*)(lds + G2_V + xrow * 128 + (((hp * 8 + 2 * g + h) ^ fv) * 8)) = u;
  }
  softmax16(pk0, sc);
  softmax16(pk1, sc);
#pragma unroll
  for (int g = 0; g < 4; g++) {
    u32x2 u = {pk(pk0[4 * g], pk0[4 * g + 1]), pk(pk0[4 * g + 2], pk0[4 * g + 3])};
    *(LAS u32x2*)(lds + G2_PK + xrow * 256 + ((((hp * 2) * 8 + 2 * g + h) ^ fpk) * 8)) = u;
    u32x2 u2 = {pk(pk1[4 * g], pk1[4 * g + 1]), pk(pk1[4 * g + 2], pk1[4 * g + 3])};
    *(LAS u32x2*)(lds + G2_PK + xrow * 256 + ((((hp * 2 + 1) * 8 + 2 * g + h) ^ fpk) * 8)) = u2;
  }
  lds_arrive(lds + L_CTL + 8, 8);
  f32x16 pq0 = mfma(wf[0], pack8(acc[0], 0), z16), pq1 = mfma(wf[1], pack8(acc[0], 1), z16);
  STAMP(5);
  if (w < 4) {
    unsigned aa[2], ba[2], bs = blk ? 0 : 2048;
#pragma unroll
    for (int tt = 0; tt < 2; tt++) {
      unsigned sr = 8 * h + 4 * tt + q;
      unsigned f1 = ((sr & 3) << 3) | ((sr >> 1) & 7), f2 = ((sr << 1) | ((sr >> 3) & 1)) & 15;
      aa[tt] = lds + G2_PK + sr * 256 + (((w * 8 + blk * 4 + pp) ^ f1) * 8);
      ba[tt] = blk ? lds + G2_ONES + pp * 8 : lds + G2_V + sr * 128 + (((w * 4 + pp) ^ f2) * 8);
    }
    bf16x8 ka[8], kb[8];
    lds_wait(lds + L_CTL + 8, 8);
#pragma unroll
    for (int ks = 0; ks < 8; ks++) {
      ka[ks] = trd2(aa[0] + ks * 4096, aa[1] + ks * 4096);
      kb[ks] = trd2(ba[0] + ks * bs, ba[1] + ks * bs);
    }
    SB0();
    f32x16 kv = {};
#pragma unroll
    for (int ks = 0; ks < 8; ks++) kv = mfma(ka[ks], kb[ks], kv);
    const int head = hg * 4 + w, c = l & 31;
    const unsigned off = c < 16 ? WS_KVP + (rt * 32 + head) * 1024 + (h * 32 + c) * 16 : c == 16 ? WS_ZP + head * 1024 + (h * 32 + rt) * 16 : OOB;
    st16<SC1>(pack8u(kv, 0), rw, off);
    st16<SC1>(pack8u(kv, 1), rw, off + 256);
    drain();
    if (lds_arrive(lds + L_CTL, 4)) {
      if (l == 0) put_flag(rw, WS_F2 + (hg * 16 + rt) * 128, lds_tag(lds));
    }
  }
  softmax16(pq0, sc);
  softmax16(pq1, sc);
  bf16x8 phq[2][2] = {{pack8(pq0, 0), pack8(pq0, 1)}, {pack8(pq1, 0), pack8(pq1, 1)}};
  lds_wait(lds + L_CTL + 12, 1);
  STAMP(8);
  {
    const int hh = w & 3, half = w >> 2, head = hg * 4 + hh;
    u32x4 v[8];
#pragma unroll
    for (int i = 0; i < 8; i++) v[i] = ld16<SC1>(rw, l * 16 + ((half * 8 + i) * 32 + head) * 1024, WS_KVP);
    u32x4 zv = ld16<SC1>(rw, l * 16 + head * 1024, half == 0 ? WS_ZP : OOB);
    float s[8] = {};
    unsigned sel_lo = 0x00003f80u;
    asm("" : "+s"(sel_lo));
#pragma unroll
    for (int i = 0; i < 8; i++)
#pragma unroll
      for (int jj = 0; jj < 4; jj++) { s[2 * jj] = dot2(v[i][jj], sel_lo, s[2 * jj]); s[2 * jj + 1] = dot2(v[i][jj], 0x3f800000u, s[2 * jj + 1]); }
    u32x4 o = {pk(s[0], s[1]), pk(s[2], s[3]), pk(s[4], s[5]), pk(s[6], s[7])};
    const unsigned ta = lds + G2_T + (half * 4 + hh) * 2048 + ((l >> 4) & 1) * 1024 + ((l >> 5) * 32 + (l & 15)) * 16;
    *(LAS u32x4*)ta = o;
    *(LAS u32x4*)(ta + 256) = zv;
  }
  lds_arrive(lds + L_CTL + 16, 8);
  lds_wait(lds + L_CTL + 16, 8);
  bf16x8 ta_[2][4];
#pragma unroll
  for (int e = 0; e < 2; e++)
#pragma unroll
    for (int c = 0; c < 4; c++) ta_[e][c] = lds16(lds + G2_T + ((c >> 1) * 4 + hp * 2 + e) * 2048 + (c & 1) * 1024 + l * 16);
  SB0();
  f32x16 a[2] = {};
#pragma unroll
  for (int c = 0; c < 4; c++)
#pragma unroll
    for (int e = 0; e < 2; e++) a[e] = mfma(ta_[e][c], phq[e][c & 1], a[e]);
#pragma unroll
  for (int e = 0; e < 2; e++) {
    float den = ((a[e][8] + a[e][9]) + (a[e][10] + a[e][11])) + ((a[e][12] + a[e][13]) + (a[e][14] + a[e][15]));
    den = __builtin_amdgcn_rcpf(half_sum(den) + 1e-6f);
#pragma unroll
    for (int i = 0; i < 8; i++) a[e][i] *= den;
    st16<SC1>(pack8u(a[e], 0), rw, WS_O + l * 16 + ((rt * 4 + mq) * 32 + hg * 4 + hp * 2 + e) * 1024);
  }
  STAMP(15);
}

DI void p2_touch(const Params& p, int st, int dt) {
  const int l = threadIdx.x & 63;
  rsrc_t rw2 = mk(p.w2, F * S * 2), rwo = mk(p.wo, D * D * 2);
  unsigned sink = 0;
#pragma nounroll
  for (int k = 0; k < 11; k++) sink += __builtin_amdgcn_raw_buffer_load_b32(rw2, (l >> 3) * 4096 + (l & 7) * 128, (k * 16 + (dt & 1) * 8 + (st & 7)) * (8 * 4096) + (st >> 3) * 1024, 0);
#pragma nounroll
  for (int k = 0; k < 2; k++) sink += __builtin_amdgcn_raw_buffer_load_b32(rwo, (l >> 2) * 1024 + (l & 3) * 128, (k * 16 + (dt & 1) * 8 + (st & 7)) * (16 * 1024) + (dt >> 2) * 512, 0);
  asm volatile("" ::"v"(sink));
}
DI void p2_loader(const Params& p, unsigned lds, int st, int dt, int j, bool g1t) {
  const int l = threadIdx.x & 63;
  rsrc_t rwo = mk(p.wo, D * D * 2), rw = mk(p.ws, WS_BYTES);
  const unsigned tvo = (j * 32 + (l >> 3)) * 1024 + ((l & 7) ^ (((l >> 4) & 1) << 2)) * 16, base = lds + j * 4096;
  const TW tw(p, lds, st, dt, j);
  const int rot = tw.rot;
  wvb<0>();
  STAMPW(6, j == 0);
  tw(6, 6); tw(7, g1t ? 4 : 7); tw(8, g1t ? 5 : 8);
  bar();
  tw(9, g1t ? 7 : 4); tw(10, g1t ? 8 : 5);
  wvb<0>();
  STAMPW(7, j == 0);
#pragma nounroll
  for (int s = 0; s < 4; s++) {
    const unsigned to = ((s + rot) & 3) * (128 * 1024) + dt * 128;
#pragma unroll
    for (int i = 0; i < 4; i++) dma16(rwo, tvo, to + i * (8 * 1024), base + s * P2_S + i * 1024);
  }
  STAMPW(14, j == 0);
  bar();
  STAMPW(10, j == 0);
  int slot = 4;
  auto issue = [&](int s) {
    const unsigned lo = s < 11 ? WS_G + ((dt * 2 + (j >> 1)) * GT + ((s + rot) % 11) * 8) * 1024 : WS_O + ((st * 2 + (j >> 1)) * 32 + ((s - 11 + rot) & 3) * 8) * 1024;
    dma16x4_sc1(rw, l * 16, lo + (j & 1) * 4096, base + slot * P2_S);
    slot = slot == 8 ? 4 : slot + 1;
  };
  issue(0); issue(1);
  wvb<4>();
  issue(2); issue(3); issue(4);
  wvb<12>();
#pragma nounroll
  for (int s = 1; s < 11; s++) {
    issue(s + 4);
    wvb<12>();
  }
  wvb<8>(); wvb<4>(); wvb<0>();
  bar();
  bar();
}
DI void p2_compute(const Params& p, unsigned lds, int st, int dt, int w, unsigned pflag, bool g1t) {
  const int t = threadIdx.x, l = t & 63;
  rsrc_t rx = mk(p.x, S * D * 2), rb2 = mk(p.b2, S * 2), ro = mk(p.out, S * D * 2);
  const unsigned ov = (t >> 3) * 1024 + (t & 7) * 16 + st * 65536 + dt * 128;
  const int h = l >> 5, blk = (l >> 4) & 1, q = (l & 15) >> 2, pp = l & 3;
  const int kq = w >> 1, ms = w & 1;
  const unsigned t0 = lds + (kq * 32 + 4 * h + q) * 128 + (((blk * 2 + (pp >> 1)) ^ (((q >> 1) & 1) << 2)) * 16) + (pp & 1) * 8;
  const unsigned tm = t0 ^ (ms * 64), t1 = t0 ^ 64;
  bf16x8 tw[11][2];
  auto pull = [&](int s, unsigned a) {
    tw[s][0] = trd2(a, a + 1024);
    tw[s][1] = trd2(a + 2048, a + 3072);
  };
  const unsigned ta = tm + (g1t ? 7 : 4) * P2_S, tb = tm + (g1t ? 4 : 7) * P2_S;
  bar();
#pragma unroll
  for (int s = 0; s < 4; s++) pull(s, tm + s * P2_S);
  pull(4, ta); pull(5, ta + P2_S);
  rsrc_t rw = mk(p.ws, WS_BYTES);
  if (pflag) {
    drain();
    if (lds_arrive(lds + L_CTL + 56, g1t ? 6 : 8)) {
      if (l == 0) put_flag(rw, pflag, lds_tag(lds));
    }
    STAMP(9);
  }
  bar();
  u32x4 xres = ld16(rx, ov, 0);
  unsigned short b2v = __builtin_amdgcn_raw_buffer_load_b16(rb2, (t >> 3) * 2, st * 128, 0);
  bar();
  pull(6, tm + 6 * P2_S); pull(7, tb); pull(8, tb + P2_S); pull(9, ta); pull(10, ta + P2_S);
  if (w == 7) poll_flags(rw, (l & 31) < 15 ? WS_F3 + (dt * 16 + (l & 31)) * 128 : WS_F4 + ((st >> 1) * 8 + (l & 7)) * 128, lds_tag(lds));
  bar();
  const unsigned la = lds + 4 * P2_S + kq * 2048 + l * 16, la4 = la + 4 * P2_S;
  f32x16 acc[2] = {};
  bf16x8 fa[2][2][2], fo[2][2];
  auto rd = [&](int s, int k) {
    const int buf = s & 1, sl = s % 5;
    const unsigned a = (sl < 4 ? la + sl * P2_S : la4) + k * 1024;
    if (s < 11) {
      fa[buf][k][0] = lds16(a);
      fa[buf][k][1] = lds16(a + 8192);
    } else {
      fo[buf][k] = lds16(a + ms * 8192);
      const unsigned o = (s - 11) * P2_S + k * 2048;
      fa[buf][k][0] = trd2(t0 + o, t0 + o + 1024);
      fa[buf][k][1] = trd2(t1 + o, t1 + o + 1024);
    }
  };
  bar();
  STAMP(11);
  rd(0, 0); rd(0, 1);
#pragma unroll
  for (int s = 0; s < 15; s++) {
    bar();
#pragma unroll
    for (int k = 0; k < 2; k++) {
      const bf16x8 bop = s < 11 ? tw[s < 11 ? s : 0][k] : fo[s & 1][k];
      acc[0] = mfma(fa[s & 1][k][0], bop, acc[0]);
      SB0();
      if (s < 14 && k == 0) { rd(s + 1, 0); rd(s + 1, 1); }
      SB0();
      acc[1] = mfma(fa[s & 1][k][1], bop, acc[1]);
      SB0();
    }
  }
  STAMP(12);
#pragma unroll
  for (int nd = 0; nd < 2; nd++)
#pragma unroll
    for (int g = 0; g < 4; g++) {
      f32x4 v = {acc[nd][4 * g], acc[nd][4 * g + 1], acc[nd][4 * g + 2], acc[nd][4 * g + 3]};
      *(LAS f32x4*)(lds + kq * 17408 + (ms * 32 + (l & 31)) * 272 + (nd * 32 + 8 * g + 4 * h) * 4) = v;
    }
  bar();
  {
    const unsigned ra = lds + (t >> 3) * 272 + (t & 7) * 32;
    f32x4 a0 = (*(LAS f32x4*)ra + *(LAS f32x4*)(ra + 17408)) + (*(LAS f32x4*)(ra + 2 * 17408) + *(LAS f32x4*)(ra + 3 * 17408));
    f32x4 a1 = (*(LAS f32x4*)(ra + 16) + *(LAS f32x4*)(ra + 17408 + 16)) + (*(LAS f32x4*)(ra + 2 * 17408 + 16) + *(LAS f32x4*)(ra + 3 * 17408 + 16));
    const float bb = blo(b2v);
    u32x4 o = {pk(a0[0] + bb + blo(xres[0]), a0[1] + bb + bhi(xres[0])), pk(a0[2] + bb + blo(xres[1]), a0[3] + bb + bhi(xres[1])),
               pk(a1[0] + bb + blo(xres[2]), a1[1] + bb + bhi(xres[2])), pk(a1[2] + bb + blo(xres[3]), a1[3] + bb + bhi(xres[3]))};
    st16(o, ro, ov);
  }
  STAMP(13);
}

extern __shared__ __attribute__((aligned(128))) char smem[];
DI unsigned lds_base() { return (unsigned)(size_t)(LAS char*)smem; }

__global__ __launch_bounds__(NT, 3) void k_main(Params p) {
  const int b = blockIdx.x, xr = (b + 7) & 7, i = b >> 3, l = threadIdx.x & 63, w = __builtin_amdgcn_readfirstlane(threadIdx.x >> 6);
  const unsigned lds = lds_base();
  unsigned long long pc0;
  asm volatile("s_getpc_b64 %0" : "=s"(pc0));
  pc0 &= ~127ull;
  STAMP(0);
  rsrc_t rw = mk(p.ws, WS_BYTES);
  const int st = (xr >> 1) * 8 + (i & 7), dt2 = (xr & 1) * 4 + (i >> 3);
  const bool g1 = i < 16;
  const int gx = xr & 3;
  const bool crew = g1 && (w < 6 || w >= 10);
  const int c = w < 6 ? w : w - 4, lb = xr * 16 + (i & 15);
  u32x4 v0, v1;
  if (crew) {
    if (CPRIO) __builtin_amdgcn_s_setprio(CPRIO);
    rsrc_t rx = mk(p.x, S * D * 2);
    const unsigned o = (lb * 16 + c * 2 + (l >> 5)) * 1024 + (l & 31) * 16;
    v0 = ld16(rx, o, 0);
    v1 = ld16(rx, o + 512, 0);
  }
  unsigned seedv = 0;
  if (crew || w == 0) seedv = __builtin_amdgcn_raw_buffer_load_b32(rw, WS_SEED, 0, SC1);
  if (threadIdx.x < 128) *(LAS unsigned*)(lds + L_CTL + threadIdx.x * 4) = 0u;
  bar();
  if (w == 0) { *(volatile LAS unsigned*)(lds + L_CTL + 24) = seedv + 1u; lds_set(lds + L_CTL + 52); }
  unsigned pflag = 0;
  bool g1t = false;
  if (g1) {
    const int dt = (xr & 1) * 4 + (i & 3), ft = (xr >> 1) * 4 + (i >> 2);
    const int rot = (4 * (i & 3) + 5 * (i >> 2) + 2 * xr) & 15;
    const bool bumper = xr == 6 && i == 12;
    if (!crew) {
      {
        rsrc_t rc = mk((const void*)pc0, CODE_BYTES);
        unsigned cv = __builtin_amdgcn_raw_buffer_load_b32(rc, ((w - 6) * 64 + l) * 128, 0, 0);
        asm volatile("" ::"v"(cv));
      }
      if (ft >= 14) cs_phase(p, lds, (ft - 14) * 8 + dt, w - 6, 0);
      if (ft == 15) cs_phase(p, lds, 16 + dt, w - 6, 1);
      if (ft < 15) g1_wloader(p, lds, ft, rot, w - 6, st, dt2);
    } else {
      const unsigned tag = __builtin_amdgcn_readfirstlane(seedv) + 1u;
      ln_phase(p, lds, lb, c, tag, v0, v1);
      STAMP(1);
      if (ft < 15) {
        if (w < 6) { if (CPRIO) __builtin_amdgcn_s_setprio(0); g1_compute(p, lds, dt, ft, w, tag); pflag = WS_F3 + (dt * 16 + ft) * 128; STAMP(5); }
        else { g1_xloader(p, lds, dt, rot, w - 10, tag); bar(); }
      } else if (bumper && w == 0) {
        poll_ln(rw, tag);
        poll_flags(rw, WS_F2 + l * 128, tag);
        poll_flags(rw, WS_F2 + 8192 + l * 128, tag);
        if (l == 0) __builtin_amdgcn_raw_buffer_store_b32(tag, rw, WS_SEED, 0, SC1);
      }
    }
    g1t = ft < 15;
    if (!g1t) {
      bar();
      if (w >= 8) {
        const TW tw(p, lds, st, dt2, w - 8);
#pragma nounroll
        for (int s = 0; s < 6; s++) tw(s, s);
      }
    }
  } else {
    const int rt = (xr >> 1) * 4 + (i & 3), hg = (xr & 1) * 4 + ((i >> 2) & 3), rot = ((i & 3) * 2 + ((i >> 2) & 3) * 3) & 7;
    if (w >= 8) {
      g2_loader(p, lds, rt, hg, rot, w - 8);
      if (w == 11 && T_GATE) {
        lds_wait(lds + L_CTL, 4);
        poll_flags(rw, WS_F2 + (hg * 16 + (l & 15)) * 128, lds_tag(lds));
        lds_set(lds + L_CTL + 12);
      }
      if (T_GATE) lds_wait(lds + L_CTL + T_GATE, 8);
      const TW tw(p, lds, st, dt2, w - 8);
#pragma nounroll
      for (int s = 0; s < 6; s++) { tw(s, s); wv<T_PACE>(); if (T_SLEEP) __builtin_amdgcn_s_sleep(T_SLEEP); }
      STAMPW(1, w == 8);
      if (w == 11 && !T_GATE) {
        lds_wait(lds + L_CTL, 4);
        poll_flags(rw, WS_F2 + (hg * 16 + (l & 15)) * 128, lds_tag(lds));
        lds_set(lds + L_CTL + 12);
      }
    } else {
      g2_compute(p, lds, rt, hg, w);
      pflag = WS_F4 + (rt * 8 + hg) * 128;
    }
  }
  if (w < 8) p2_compute(p, lds, st, dt2, w, pflag, g1t);
  else p2_loader(p, lds, st, dt2, w - 8, g1t);
}
}

extern "C" void kernel_launch(void* const* d_in, const int* in_sizes, int n_in, void* d_out, int out_size, void* d_ws, size_t ws_size,
                              hipStream_t stream) {
  using namespace hk;
  Params p{d_in[0], d_in[1], d_in[2], d_in[3], d_in[5], d_in[6], d_in[7], d_in[8], d_out, d_ws};
  (void)hipFuncSetAttribute((const void*)k_main, hipFuncAttributeMaxDynamicSharedMemorySize, LDS_BYTES);
  k_main<<<256, NT, LDS_BYTES, stream>>>(p);
}
```

```cpp
#include <hip/hip_runtime.h>
#include <hip/hip_bf16.h>
#define FUSE_P2 1
#define MIXED 1
#define W_EARLY 0
#define SPIN_SLEEP 1
#define POLL_SLEEP 1
#define LPRIO 0
#define WO_EARLY 1
#define LN_DELAY 12
#define KV_DELAY 16
#define G1PRIO 0
#define XROT 7
#define L_AHEAD 4
#define G1BAL 0
#define G2ROT 2
#define CPRIO 3
#define T_PACE 40
#define T_SLEEP 0
#define T_GATE 0
#define W_GATE 12
#define CODE_BYTES 0

namespace hk {
#define DI __device__ __forceinline__
#define LAS __attribute__((address_space(3)))
typedef short bf16x8 __attribute__((ext_vector_type(8)));
typedef short s16x4 __attribute__((ext_vector_type(4)));
typedef float f32x16 __attribute__((ext_vector_type(16)));
typedef float f32x4 __attribute__((ext_vector_type(4)));
typedef float f32x2 __attribute__((ext_vector_type(2)));
typedef unsigned u32x4 __attribute__((ext_vector_type(4)));
typedef unsigned u32x2 __attribute__((ext_vector_type(2)));
typedef __bf16 bfx2 __attribute__((ext_vector_type(2)));
typedef __amdgpu_buffer_rsrc_t rsrc_t;

constexpr int S = 2048, D = 512, F = 1376;
constexpr int GT = 88;
constexpr int WS_SEED = 0;
constexpr int WS_F2 = 4096;
constexpr int WS_F3 = 20480;
constexpr int WS_F4 = 36864;
constexpr int WS_CS = 53248;
constexpr int WS_SB = 65536;
constexpr int WS_ZP = 98304;
constexpr int WS_KVP = 131072;
constexpr int WS_G = 1 << 20;
constexpr int WS_O = 3 << 20;
constexpr int WS_H = 5 << 20;
constexpr int WS_BYTES = 7 << 20;
constexpr int SC1 = 16;
constexpr int NT = 768;
constexpr unsigned OOB = 0x40000000u;
constexpr int SPIN_CAP = 1 << 14;

#define STAMP(i)
#define STAMPW(i, cond)
#define FST(i)
#define FSTW(i)
#define FSTV(i, v)
struct Params {
  const void *x, *wqkv, *wphi, *wo, *w1, *b1, *w2, *b2;
  void *out, *ws;
};

#define SB0() __builtin_amdgcn_sched_barrier(0)
DI rsrc_t mk(const void* p, int bytes) { return __builtin_amdgcn_make_buffer_rsrc((void*)p, 0, bytes, 0x00020000); }
DI void dma16(rsrc_t r, unsigned voff, unsigned soff, unsigned lds) {
  asm volatile("s_mov_b32 m0, %0\n\ts_nop 0\n\tbuffer_load_dwordx4 %1, %2, %3 offen lds" ::"s"(lds), "v"(voff), "s"(r), "s"(soff) : "memory");
}
DI void dma16_sc1(rsrc_t r, unsigned voff, unsigned soff, unsigned lds) {
  asm volatile("s_mov_b32 m0, %0\n\ts_nop 0\n\tbuffer_load_dwordx4 %1, %2, %3 offen sc1 lds" ::"s"(lds), "v"(voff), "s"(r), "s"(soff) : "memory");
}
DI void dma16x4_sc1(rsrc_t r, unsigned voff, unsigned soff, unsigned lds) {
  asm volatile("s_mov_b32 m0, %0\n\ts_nop 0\n\tbuffer_load_dwordx4 %1, %2, %3 offen sc1 lds\n\tbuffer_load_dwordx4 %1, %2, %3 offen offset:1024 sc1 lds\n\t"
               "buffer_load_dwordx4 %1, %2, %3 offen offset:2048 sc1 lds\n\tbuffer_load_dwordx4 %1, %2, %3 offen offset:3072 sc1 lds" ::"s"(lds), "v"(voff), "s"(r), "s"(soff) : "memory");
}
DI unsigned lds_tag(unsigned lds);
DI void lds_set(unsigned a) { *(volatile LAS unsigned*)a = 1u; }
template <int N> DI void wvb() { asm volatile("s_waitcnt vmcnt(%0) lgkmcnt(0)\n\ts_barrier" ::"i"(N) : "memory"); }
DI void bar() { asm volatile("s_waitcnt lgkmcnt(0)\n\ts_barrier" ::: "memory"); }
DI void drain() { asm volatile("s_waitcnt vmcnt(0)" ::: "memory"); }
template <int AUX = 0> DI u32x4 ld16(rsrc_t r, unsigned voff, unsigned soff) { return __builtin_amdgcn_raw_buffer_load_b128(r, voff, soff, AUX); }
template <int AUX = 0> DI u32x2 ld8(rsrc_t r, unsigned voff, unsigned soff) { return __builtin_amdgcn_raw_buffer_load_b64(r, voff, soff, AUX); }
template <int AUX = 0> DI void st16(u32x4 v, rsrc_t r, unsigned voff) { __builtin_amdgcn_raw_buffer_store_b128(v, r, voff, 0, AUX); }
DI s16x4 trd(unsigned a) { return __builtin_amdgcn_ds_read_tr16_b64_v4i16((LAS s16x4*)a); }
DI bf16x8 trd2(unsigned a0, unsigned a1) {
  s16x4 lo = trd(a0), hi = trd(a1);
  return __builtin_shufflevector(lo, hi, 0, 1, 2, 3, 4, 5, 6, 7);
}
DI bf16x8 lds16(unsigned a) { return *(LAS bf16x8*)a; }
DI f32x16 mfma(bf16x8 a, bf16x8 b, f32x16 c) { return
 __builtin_amdgcn_mfma_f32_32x32x16_bf16(a, b, c, 0, 0, 0); }
DI unsigned pk(float lo, float hi) {
  f32x2 v = {lo, hi};
  return __builtin_bit_cast(unsigned, __builtin_convertvector(v, bfx2));
}
DI float asf(unsigned u) { return __builtin_bit_cast(float, u); }
DI unsigned asu(float u) { return __builtin_bit_cast(unsigned, u); }
DI float blo(unsigned u) { return asf(u << 16); }
DI float bhi(unsigned u) { return asf(u & 0xffff0000u); }
DI u32x4 pack8u(const f32x16& x, int s) {
  u32x4 p = {pk(x[8 * s], x[8 * s + 1]), pk(x[8 * s + 2], x[8 * s + 3]), pk(x[8 * s + 4], x[8 * s + 5]), pk(x[8 * s + 6], x[8 * s + 7])};
  return p;
}
DI bf16x8 pack8(const f32x16& x, int s) { return __builtin_bit_cast(bf16x8, pack8u(x, s)); }
DI float dot2(unsigned a, unsigned b, float c) { return __builtin_amdgcn_fdot2_f32_bf16(__builtin_bit_cast(bfx2, a), __builtin_bit_cast(bfx2, b), c, false); }
DI float half_sum(float v) {
  unsigned u = asu(v);
  auto r = __builtin_amdgcn_permlane32_swap(u, u, false, false);
  return asf(r[0]) + asf(r[1]);
}
template <int C> DI float dpp_add(float v) { return v + __builtin_amdgcn_update_dpp(0.f, v, C, 0xf, 0xf, false); }
DI float half32_sum(float v) {
  v = dpp_add<0x128>(v);
  v = dpp_add<0x124>(v);
  v = dpp_add<0x122>(v);
  v = dpp_add<0x121>(v);
  unsigned u = asu(v);
  auto r = __builtin_amdgcn_permlane16_swap(u, u, false, false);
  return asf(r[0]) + asf(r[1]);
}
DI void put_flag(rsrc_t rw, unsigned off, unsigned tag) {
  u32x2 f = {tag, ~tag};
  __builtin_amdgcn_raw_buffer_store_b64(f, rw, off, 0, SC1);
}
DI void poll_flags(rsrc_t rw, unsigned off, unsigned tag) {
#pragma nounroll
  for (int sp = 0; sp < SPIN_CAP; sp++) {
    asm volatile("" ::: "memory");
    u32x2 f = ld8<SC1>(rw, off, 0);
    if (__all(f[0] == tag && f[1] == ~tag)) break;
    __builtin_amdgcn_s_sleep(1);
  }
  asm volatile("" ::: "memory");
}
DI bool lds_arrive(unsigned cnt, unsigned n) {
  asm volatile("s_waitcnt lgkmcnt(0)" ::: "memory");
  unsigned old = 0;
  if ((threadIdx.x & 63) == 0) old = __hip_atomic_fetch_add((LAS unsigned*)cnt, 1u, __ATOMIC_RELAXED, __HIP_MEMORY_SCOPE_WORKGROUP);
  old = __builtin_amdgcn_readfirstlane(old);
  asm volatile("" ::: "memory");
  return old == n - 1;
}
DI void lds_wait(unsigned cnt, unsigned n) {
  while (__hip_atomic_load((LAS unsigned*)cnt, __ATOMIC_RELAXED, __HIP_MEMORY_SCOPE_WORKGROUP) < n) __builtin_amdgcn_s_sleep(SPIN_SLEEP);
  asm volatile("" ::: "memory");
}

DI void prog_put(unsigned a, unsigned v) { asm volatile("" ::: "memory"); *(volatile LAS unsigned*)a = v; asm volatile("" ::: "memory"); }
DI void prog_wait(unsigned a, unsigned n) {
  unsigned seen;
  asm volatile(
      ".Lpw_loop%=:\n\tds_read_b32 %0, %1\n\ts_waitcnt lgkmcnt(0)\n\t"
      "v_cmp_le_u32 vcc, %2, %0\n\ts_cmp_eq_u64 vcc, exec\n\ts_cbranch_scc0 .Lpw_loop%="
      : "=&v"(seen) : "v"(a), "s"(n) : "vcc", "scc", "memory");
}
constexpr int L_CTL = 147456;
constexpr int L_PW = L_CTL + 320;
constexpr int L_MISC = L_CTL + 512;
constexpr int LDS_BYTES = L_MISC + 9216;
constexpr int G1_W = 0, G1_WS = 24576, G1_X = 4 * G1_WS, G1_XS = 16384;
constexpr int G2_SLOT = 40960, G2_XO = 0, G2_WO = 16384, G2_PHI = L_MISC, G2_ONES = G2_PHI + 2048, G2_CS = G2_ONES + 64, G2_ST = G2_CS + 768;
constexpr int G2_PK = 98304, G2_V = G2_PK + 32768, G2_T = G2_V;
constexpr int P2_T = 0, P2_L = 98304, P2_S = 16384;
static_assert(G2_ST + 1024 <= LDS_BYTES && LDS_BYTES <= 163840, "lds");

DI unsigned lds_tag(unsigned lds) { return __builtin_amdgcn_readfirstlane(*(volatile LAS unsigned*)(lds + L_CTL + 24)); }

DI void ln_phase(const Params& p, unsigned lds, int lb, int c, unsigned tag, u32x4 v0, u32x4 v1) {
  const int l = threadIdx.x & 63;
  rsrc_t rw = mk(p.ws, WS_BYTES);
  float s = 0.f, s1 = 0.f;
#pragma unroll
  for (int i = 0; i < 4; i++) { s = dot2(v0[i], 0x3f803f80u, s); s1 = dot2(v1[i], 0x3f803f80u, s1); }
  const float m = half32_sum(s + s1) * (1.0f / D);
  f32x2 e[8], se = {0.f, 0.f}, sq = {0.f, 0.f};
#pragma unroll
  for (int i = 0; i < 4; i++) {
    e[i] = f32x2{blo(v0[i]), bhi(v0[i])} - m;
    e[4 + i] = f32x2{blo(v1[i]), bhi(v1[i])} - m;
  }
#pragma unroll
  for (int i = 0; i < 8; i++) { se += e[i]; sq += e[i] * e[i]; }
  const float me = half32_sum(se[0] + se[1]) * (1.0f / D), var = half32_sum(sq[0] + sq[1]) * (1.0f / D);
  const float r = __builtin_amdgcn_rsqf(var + 1e-5f);
  const float m2 = me * r, v2 = fmaxf(var - me * me, 0.f) * r * r;
  const float r2 = __builtin_amdgcn_rsqf(v2 + 1e-5f);
  const float ca = r * r2, cb = -m2 * r2;
  u32x4 o0, o1;
#pragma unroll
  for (int i = 0; i < 4; i++) {
    f32x2 u = e[i] * ca + cb, u2 = e[4 + i] * ca + cb;
    o0[i] = pk(u[0], u[1]);
    o1[i] = pk(u2[0], u2[1]);
  }
  const int row = c * 2 + (l >> 5);
  const unsigned ho = WS_H + ((l & 31) >> 3) * (S * 128) + (lb * 16 + row) * 128 + (((l & 7) ^ ((c & 1) << 2)) * 16);
  st16<SC1>(o0, rw, ho);
  st16<SC1>(o1, rw, ho + 4 * (S * 128));
  if ((l & 31) == 0) {
    u32x4 g = {asu(r), tag, asu(m), tag};
    *(LAS u32x4*)(lds + L_CTL + 64 + row * 16) = g;
  }
  if (W_GATE == 0) lds_arrive(lds + L_CTL, 8);
  drain();
  if (lds_arrive(lds + L_CTL + 4, 8)) {
    if (l < 16) {
      u32x4 g = *(LAS u32x4*)(lds + L_CTL + 64 + l * 16);
      st16<SC1>(g, rw, WS_SB + lb * 256 + l * 16);
    }
    lds_set(lds + L_CTL + 12);
    STAMPW(8, true);
    STAMPW(15, true);
  }
}
DI void poll_ln(rsrc_t rw, unsigned tag) {
  const int l = threadIdx.x & 63;
  bool ok0 = false, ok1 = false;
#pragma nounroll
  for (int sp = 0; sp < SPIN_CAP; sp++) {
    asm volatile("" ::: "memory");
    u32x4 f0 = ld16<SC1>(rw, ok0 ? OOB : WS_SB + l * 256, 0), f1 = ld16<SC1>(rw, ok1 ? OOB : WS_SB + 16384 + l * 256, 0);
    ok0 = ok0 || (f0[1] == tag && f0[3] == tag);
    ok1 = ok1 || (f1[1] == tag && f1[3] == tag);
    if (__all(ok0 && ok1)) break;
    __builtin_amdgcn_s_sleep(POLL_SLEEP);
  }
  asm volatile("" ::: "memory");
}

DI int g1_stage(int s, int rot) { return (s + rot) & 15; }
DI int p2_rot(int st, int dt) { return (((st & 7) * 11) >> 3) + 6 * (dt & 1) + 3 * ((dt >> 1) & 1); }
struct TW {
  rsrc_t rw2;
  unsigned tv2, base;
  int rot, st;
  DI TW(const Params& p, unsigned lds, int st_, int dt, int j) {
    const int l = threadIdx.x & 63;
    rw2 = mk(p.w2, F * S * 2);
    tv2 = (j * 32 + (l >> 3)) * 4096 + ((l & 7) ^ (((l >> 4) & 1) << 2)) * 16;
    base = lds + j * 4096; rot = p2_rot(st_, dt); st = st_;
  }
  DI void operator()(int s, int slot) const {
    const unsigned to = ((s + rot) % 11) * (128 * 4096) + st * 128;
#pragma unroll
    for (int i = 0; i < 4; i++) dma16(rw2, tv2, to + i * (8 * 4096), base + slot * P2_S + i * 1024);
  }
};
struct TP {
  bf16x8 f[11][2];
};
DI unsigned p2_t0(unsigned lds, int w) {
  const int l = threadIdx.x & 63, h = l >> 5, blk = (l >> 4) & 1, q = (l & 15) >> 2, pp = l & 3;
  return lds + ((w >> 1) * 32 + 4 * h + q) * 128 + (((blk * 2 + (pp >> 1)) ^ (((q >> 1) & 1) << 2)) * 16) + (pp & 1) * 8;
}
DI void pull(TP& t, int s, unsigned a) {
  t.f[s][0] = trd2(a, a + 1024);
  t.f[s][1] = trd2(a + 2048, a + 3072);
}
DI void pull03(TP& t, unsigned tm, bool g1t) {
  pull(t, 0, tm); pull(t, 1, tm + P2_S); pull(t, 2, tm + 2 * P2_S); pull(t, 3, tm + (g1t ? 7 : 4) * P2_S);
}
template <int N> DI void wv() { asm volatile("s_waitcnt vmcnt(%0)" ::"i"(N) : "memory"); }
DI void g1_wloader(const Params& p, unsigned lds, int ft, int rot, int j, int st, int dt2) {
  const int l = threadIdx.x & 63;
  rsrc_t rw1 = mk(p.w1, S * F * 2);
  unsigned wvo[3];
#pragma unroll
  for (int i = 0; i < 3; i++) {
    unsigned u = i * 64 + l;
    unsigned row = u / 12, ch = u - row * 12;
    wvo[i] = (ft * 96 + ch * 8 >= F) ? OOB : (j * 32 + row) * (F * 2) + ch * 16;
  }
  const unsigned wl = lds + G1_W + j * 6144;
  auto issue = [&](int q) {
    const unsigned so = g1_stage(q, rot) * (128 * F * 2) + ft * 192, dst = wl + (q & 3) * G1_WS;
#pragma unroll
    for (int k = 0; k < 6; k++) dma16(rw1, wvo[k % 3], so + (k / 3) * (16 * F * 2), dst + k * 1024);
  };
#pragma nounroll
  for (int q = 0; q < 4; q++) {
    if (q == W_EARLY) lds_wait(lds + L_CTL + W_GATE, W_GATE ? 1 : 8);
    issue(q);
  }
  wvb<18>();
  wvb<12>();
#pragma nounroll
  for (int q = 1; q < 13; q++) {
    issue(q + 3);
    wvb<12>();
  }
  const TW tw(p, lds, st, dt2, j);
  tw(0, 0);
  wvb<10>();
  tw(1, 1); tw(2, 2); tw(3, 7);
  wvb<16>();
  tw(4, 3); tw(5, 8);
  wvb<8>();
}
DI void g1_xloader(const Params& p, unsigned lds, int dt, int rot, int j, unsigned tag) {
  const int l = threadIdx.x & 63;
  rsrc_t rw = mk(p.ws, WS_BYTES);
  auto issue = [&](int n, int slot) {
    const unsigned so = WS_H + dt * (S * 128) + g1_stage(n, rot) * 16384 + j * 8192, xd = lds + G1_X + slot * G1_XS + j * 8192;
    dma16x4_sc1(rw, l * 16, so, xd);
    dma16x4_sc1(rw, l * 16, so + 4096, xd + 4096);
  };
  if (j == 0) { lds_wait(lds + L_CTL + 12, 1);
__builtin_amdgcn_s_sleep(LN_DELAY); poll_ln(rw, tag);
 lds_set(lds + L_CTL + 16); } else lds_wait(lds + L_CTL + 16, 1);
  STAMPW(2, j == 0);
  issue(0, 0); issue(1, 1);
  wvb<8>();
  int slot = 2;
#pragma nounroll
  for (int n = 0; n < 14; n++) {
    issue(n + 2, slot);
    slot = slot == 2 ? 0 : slot + 1;
    wvb<8>();
  }
  wvb<0>();
}
template <int NP> DI void g1_loop(unsigned a0, unsigned b0, f32x16 (&acc)[2], bf16x8 (&fa)[4], bf16x8 (&fx)[4][2]) {
  unsigned aa = a0, ba = b0, bb = b0 ^ 64, xs = 0;
  auto rda = [&](int k, int ks) { fa[k] = trd2(aa + ks * 3072, aa + ks * 3072 + 768); fx[k][0] = trd2(ba + ks * 2048, ba + ks * 2048 + 512); };
  auto rdb = [&](int k, int ks) { fx[k][1] = trd2(bb + ks * 2048, bb + ks * 2048 + 512); };
  auto pair = [&](int par, int nx) {
#pragma unroll
    for (int u = 0; u < 2; u++) {
      const int k = 2 * par + u;
      acc[0] = mfma(fa[k], fx[k][0], acc[0]);
      SB0();
      rda(k ^ 2, 2 * nx + u);
      SB0();
      acc[1] = mfma(fa[k], fx[k][1], acc[1]);
      SB0();
      rdb(k ^ 2, 2 * nx + u);
      SB0();
    }
  };
  auto head = [&](int par) {
#pragma unroll
    for (int i = 0; i + 1 < NP; i++) pair((par + i) & 1, i + 1);
  };
  auto body = [&](int par, int n) {
    head(par);
    xs = xs == 2 * G1_XS ? 0 : xs + G1_XS;
    aa = a0 + (n & 3) * G1_WS; ba = b0 + xs; bb = ba ^ 64;
    bar();
    pair((par + NP - 1) & 1, 0);
  };
  rda(0, 0); rdb(0, 0); rda(1, 1); rdb(1, 1);
#pragma nounroll
  for (int n = 1; n < 15; n += 2) { body(0, n); body(NP & 1, n + 1); }
  body(0, 15);
  head(NP & 1);
  bar();
}
DI void g1_compute(const Params& p, unsigned lds, int dt, int ft, int c, TP& tp) {
  const int l = threadIdx.x & 63;
  rsrc_t rw = mk(p.ws, WS_BYTES), rb1 = mk(p.b1, F * 2);
  const int h = l >> 5, blk = (l >> 4) & 1, q = (l & 15) >> 2, pp = l & 3;
  const int nf = c % 3, kh = c / 3, k0 = kh * 4, pc = kh ? nf : nf + 3;
  const int fb = ft * 96 + nf * 32;
  const unsigned a0 = lds + G1_W + (k0 * 16 + 8 * h + q) * 192 + nf * 64 + blk * 32 + pp * 8;
  const unsigned b0 = (lds + G1_X + (k0 * 16 + 8 * h + q) * 128 + (((blk * 2 + (pp >> 1)) ^ (((q >> 1) & 1) << 2)) * 16) + (pp & 1) * 8) ^ (kh * 64);
  u32x2 bv[4];
#pragma unroll
  for (int g = 0; g < 4; g++) bv[g] = ld8(rb1, (8 * g + 4 * h + fb) * 2, 0);
  f32x16 acc[2] = {};
#pragma unroll
  for (int g = 0; g < 4; g++) { acc[0][4 * g] = blo(bv[g][0]); acc[0][4 * g + 1] = bhi(bv[g][0]); acc[0][4 * g + 2] = blo(bv[g][1]); acc[0][4 * g + 3] = bhi(bv[g][1]); }
  bf16x8 fa[4], fx[4][2];
  unsigned exw = lds + 3 * G1_WS + l * 16 + c * 4096, exr = lds + 3 * G1_WS + l * 16 + pc * 4096;
  unsigned so = fb < GT * 16 ? WS_G + ((dt * 2 + kh) * GT + ft * 6 + nf * 2) * 1024 + l * 16 : OOB;
  unsigned tm = p2_t0(lds, c) ^ ((c & 1) * 64);
  asm volatile("" : "+v"(tm));
  asm volatile("" : "+v"(exw), "+v"(exr), "+v"(so), "+v"(acc[0]), "+v"(acc[1]));
  bar();
  STAMP(3);
  g1_loop<2>(a0, b0, acc, fa, fx);
  acc[1] = mfma(fa[2], fx[2][1], acc[1]);
  SB0();
  pull03(tp, tm, true);
  SB0();
  acc[1] = mfma(fa[3], fx[3][1], acc[1]);
  acc[0] = mfma(fa[2], fx[2][0], acc[0]);
  acc[0] = mfma(fa[3], fx[3][0], acc[0]);
  SB0();
  STAMP(4);
#pragma unroll
  for (int g = 0; g < 4; g++) {
    f32x4 v = {acc[1][4 * g], acc[1][4 * g + 1], acc[1][4 * g + 2], acc[1][4 * g + 3]};
    *(LAS f32x4*)(exw + g * 1024) = v;
  }
  bar();
  u32x4 o[2];
#pragma unroll
  for (int g = 0; g < 4; g++) {
    const f32x4 v = *(LAS f32x4*)(exr + g * 1024);
#pragma unroll
    for (int jj = 0; jj < 2; jj++) {
      const f32x2 u = f32x2{acc[0][4 * g + 2 * jj], acc[0][4 * g + 2 * jj + 1]} + f32x2{v[2 * jj], v[2 * jj + 1]};
      const f32x2 z = u * (u * u * -0.1029432397f + -2.302208198f);
      const f32x2 d = f32x2{__builtin_amdgcn_exp2f(z[0]), __builtin_amdgcn_exp2f(z[1])} + 1.0f;
      const f32x2 r = u * f32x2{__builtin_amdgcn_rcpf(d[0]), __builtin_amdgcn_rcpf(d[1])};
      o[g >> 1][(g & 1) * 2 + jj] = pk(r[0], r[1]);
    }
  }
  st16<SC1>(o[0], rw, so);
  st16<SC1>(o[1], rw, so + 1024);
}

DI float max3(float a, float b, float c) { float r; asm("v_max3_f32 %0, %1, %2, %3" : "=v"(r) : "v"(a), "v"(b), "v"(c)); return r; }
DI void softmax16(f32x16 p, float sc, unsigned (&o)[8]) {
  asm volatile("s_nop 11" : "+v"(p));
  float m0 = max3(p[0], p[1], p[2]), m1 = max3(p[3], p[4], p[5]), m2 = max3(p[6], p[7], p[8]), m3 = max3(p[9], p[10], p[11]), m4 = max3(p[12], p[13], p[14]);
  const float nmx = -max3(max3(m0, m1, p[15]), max3(m2, m3, m4), m0) * sc;
  f32x2 e[8];
#pragma unroll
  for (int i = 0; i < 8; i++) {
    const f32x2 z = f32x2{p[2 * i], p[2 * i + 1]} * sc + nmx;
    e[i] = f32x2{__builtin_amdgcn_exp2f(z[0]), __builtin_amdgcn_exp2f(z[1])};
  }
  const f32x2 s2 = ((e[0] + e[1]) + (e[2] + e[3])) + ((e[4] + e[5]) + (e[6] + e[7]));
  const float s = __builtin_amdgcn_rcpf(s2[0] + s2[1]);
#pragma unroll
  for (int i = 0; i < 8; i++) { const f32x2 r = e[i] * s; o[i] = pk(r[0], r[1]); }
}
template <bool SC> DI void center(const f32x16& a, const f32x4 (&c)[4], float nm, float r, unsigned (&o)[8]) {
#pragma unroll
  for (int i = 0; i < 8; i++) {
    f32x2 v = f32x2{c[i >> 1][(i & 1) * 2], c[i >> 1][(i & 1) * 2 + 1]} * nm + f32x2{a[2 * i], a[2 * i + 1]};
    if (SC) v = v * r;
    o[i] = pk(v[0], v[1]);
  }
}
DI bf16x8 frag(const unsigned (&o)[8], int s) { u32x4 u = {o[4 * s], o[4 * s + 1], o[4 * s + 2], o[4 * s + 3]}; return __builtin_bit_cast(bf16x8, u); }

DI void g2_loader(const Params& p, unsigned lds, int rt, int hg, int rot, int j) {
  const int l = threadIdx.x & 63;
  rsrc_t rx = mk(p.x, S * D * 2), rq = mk(p.wqkv, D * 3 * D * 2), rp = mk(p.wphi, 32 * 256 * 2);
  unsigned xv[2];
#pragma unroll
  for (int i = 0; i < 2; i++) xv[i] = (j * 32 + i * 8 + (l >> 3)) * 1024 + (((l & 7) ^ ((4 * i + (l >> 4)) & 7)) * 16);
  const unsigned wv = (j * 16 + (l >> 3)) * 3072 + (((l & 7) ^ (((l >> 4) & 1) << 2)) * 16);
  auto issue = [&](int s, int slot) {
    const unsigned sidx = (s + rot) & 7, base = lds + slot * G2_SLOT;
    const unsigned xo = rt * (128 * 1024) + sidx * 128, wo = sidx * (64 * 3072) + hg * 128;
#pragma unroll
    for (int i = 0; i < 2; i++) {
      dma16(rx, xv[0], xo + i * 16384, base + G2_XO + j * 4096 + i * 2048);
      dma16(rx, xv[1], xo + i * 16384, base + G2_XO + j * 4096 + i * 2048 + 1024);
#pragma unroll
      for (int c = 0; c < 3; c++) dma16(rq, wv, wo + i * (8 * 3072) + c * 1024, base + G2_WO + c * 8192 + j * 2048 + i * 1024);
    }
  };
  issue(0, 0);
  issue(1, 1);
  wvb<10>();
  int slot = 2;
  for (int s = 0; s < 6; s++) {
    issue(s + 2, slot);
    slot = slot == 2 ? 0 : slot + 1;
    wvb<10>();
  }
  if (j < 2) dma16(rp, l * 16 + j * 1024, hg * 2048, lds + G2_PHI + j * 1024);
  {
    rsrc_t rw = mk(p.ws, WS_BYTES);
    const unsigned tag = lds_tag(lds);
    const unsigned off = j < 2 ? WS_SB + (rt * 128 + j * 64 + l) * 16 : WS_CS + hg * 1536 + (j - 2) * 768 + (l < 48 ? l : 0) * 16;
    u32x4 g;
#pragma nounroll
    for (int sp = 0; sp < SPIN_CAP; sp++) {
      asm volatile("" ::: "memory");
      g = ld16<SC1>(rw, off, 0);
      if (__all(g[1] == tag && g[3] == tag)) break;
    }
    u32x2 o = {g[0], g[2]};
    if (j < 2 || l < 48) *(LAS u32x2*)(lds + (j < 2 ? G2_ST + j * 512 : G2_CS + (j - 2) * 384) + l * 8) = o;
  }
  wvb<0>();
}
DI void cs_phase(const Params& p, unsigned lds, int cg, int j, int pass) {
  const int l = threadIdx.x & 63;
  rsrc_t rq = mk(p.wqkv, D * 3 * D * 2), rw = mk(p.ws, WS_BYTES);
  float s[8] = {};
  unsigned sel_lo = 0x00003f80u;
  asm("" : "+s"(sel_lo));
  const unsigned vo = (j * 128 + (l >> 3)) * 3072 + (l & 7) * 16 + cg * 128;
  u32x4 v[16];
#pragma unroll
  for (int u = 0; u < 16; u++) v[u] = ld16(rq, vo, u * (8 * 3072));
  SB0();
#pragma unroll
  for (int u = 0; u < 16; u++)
#pragma unroll
    for (int jj = 0; jj < 4; jj++) { s[2 * jj] = dot2(v[u][jj], sel_lo, s[2 * jj]); s[2 * jj + 1] = dot2(v[u][jj], 0x3f800000u, s[2 * jj + 1]); }
  const unsigned sc = pass ? lds : lds + L_MISC;
  const unsigned pa = sc + (j * 8 + (l >> 3)) * 272 + (l & 7) * 32;
  f32x4 a = {s[0], s[1], s[2], s[3]}, b = {s[4], s[5], s[6], s[7]};
  *(LAS f32x4*)pa = a;
  *(LAS f32x4*)(pa + 16) = b;
  if (lds_arrive(lds + L_CTL + 28 + pass * 4, 4)) {
    float t = 0.f;
#pragma unroll
    for (int r = 0; r < 32; r++) t += *(LAS float*)(sc + r * 272 + l * 4);
    lds_wait(lds + L_CTL + 52, 1);
    u32x2 g = {asu(t), lds_tag(lds)};
    __builtin_amdgcn_raw_buffer_store_b64(g, rw, WS_CS + (cg & 7) * 1536 + (((l >> 5) * 3 + (cg >> 3)) * 32 + (l & 31)) * 8, 0, SC1);
  }
}

DI void g2_kv(const Params& p, unsigned lds, int rt, int hg, int w) {
  const int l = threadIdx.x & 63;
  rsrc_t rw = mk(p.ws, WS_BYTES);
  const int h = l >> 5, blk = (l >> 4) & 1, q = (l & 15) >> 2, pp = l & 3;
  const unsigned pwa = lds + L_PW + (l & 7) * 4;
    unsigned aa[2], ba[2], bs = blk ? 0 : 2048;
#pragma unroll
    for (int tt = 0; tt < 2; tt++) {
      unsigned sr = 8 * h + 4 * tt + q;
      unsigned f1 = ((sr & 3) << 3) | ((sr >> 1) & 7), f2 = ((sr << 1) | ((sr >> 3) & 1)) & 15;
      aa[tt] = lds + G2_PK + sr * 256 + (((w * 8 + blk * 4 + pp) ^ f1) * 8);
      ba[tt] = blk ? lds + G2_ONES + pp * 8 : lds + G2_V + sr * 128 + (((w * 4 + pp) ^ f2) * 8);
    }
    unsigned bb[8][2];
#pragma unroll
    for (int ks = 0; ks < 8; ks++) { bb[ks][0] = ba[0] + ks * bs; bb[ks][1] = ba[1] + ks * bs; asm volatile("" : "+v"(bb[ks][0]), "+v"(bb[ks][1])); }
    const int head = hg * 4 + w, c = l & 31;
    unsigned off = c < 16 ? WS_KVP + (rt * 32 + head) * 1024 + (h * 16 + c) * 16 : c == 16 ? WS_ZP + head * 1024 + (h * 16 + rt) * 16 : OOB;
    asm volatile("" : "+v"(off), "+v"(aa[0]), "+v"(aa[1]));
    bar();
    __builtin_amdgcn_s_sleep(KV_DELAY);
    __builtin_amdgcn_s_setprio(2);
    prog_wait(pwa, 1);
    FSTW(6);
    bf16x8 ka[8], kb[8];
#pragma unroll
    for (int ks = 0; ks < 8; ks++) {
      ka[ks] = trd2(aa[0] + ks * 4096, aa[1] + ks * 4096);
      kb[ks] = trd2(bb[ks][0], bb[ks][1]);
    }
    SB0();
    f32x16 kv = {};
#pragma unroll
    for (int ks = 0; ks < 8; ks++) kv = mfma(ka[ks], kb[ks], kv);

    st16<SC1>(pack8u(kv, 0), rw, off);
    st16<SC1>(pack8u(kv, 1), rw, off + 512);
    drain();
    if (lds_arrive(lds + L_CTL, 4)) {
      if (l == 0) put_flag(rw, WS_F2 + (hg * 16 + rt) * 128, lds_tag(lds));
      FSTW(14);
    }
    __builtin_amdgcn_s_setprio(0);
}

DI void g2_compute(const Params& p, unsigned lds, int rt, int hg, int w) {
  const int t = threadIdx.x, l = t & 63;
  rsrc_t rw = mk(p.ws, WS_BYTES);
  if (t < 16) *(LAS unsigned*)(lds + G2_ONES + t * 4) = 0x3f803f80u;
  const int h = l >> 5, blk = (l >> 4) & 1, q = (l & 15) >> 2, pp = l & 3;
  const int mq = w >> 1, hp = w & 1;
  const unsigned xrow = mq * 32 + (l & 31);
  unsigned xa[4];
#pragma unroll
  for (int k = 0; k < 4; k++) xa[k] = (lds + G2_XO + xrow * 128 + ((h ^ ((xrow >> 1) & 7)) * 16)) ^ (k << 5);
  const unsigned wa_ = lds + G2_WO + (8 * h + q) * 128 + (((hp * 4 + blk * 2 + (pp >> 1)) ^ (((q >> 1) & 1) << 2)) * 16) + (pp & 1) * 8;
  f32x16 acc[3] = {};
  bf16x8 fx[4], fw[4][3];
  unsigned sb = 0;
  auto rd = [&](int k, int i) {
    if (i == 0) fx[k] = lds16(xa[k] + sb);
    const unsigned wb = wa_ + sb + k * 2048 + i * 8192;
    fw[k][i] = trd2(wb, wb + 512);
  };
  auto step = [&](int k, bool more) {
#pragma unroll
    for (int i = 0; i < 3; i++) {
      acc[i] = mfma(fw[k][i], fx[k], acc[i]);
      SB0();
      if (more) rd(k ^ 2, i);
      SB0();
    }
  };
  unsigned pka = (lds + G2_PK + xrow * 256 + (((xrow & 3) << 3) | ((xrow >> 1) & 7)) * 8) ^ ((hp * 16 + h) * 8);
  unsigned va = (lds + G2_V + xrow * 128 + (((xrow << 1) | ((xrow >> 3) & 1)) & 15) * 8) ^ ((hp * 8 + h) * 8);
  unsigned csa = lds + G2_CS + (hp * 96 + 4 * h) * 4;
  asm volatile("" : "+v"(acc[0]), "+v"(acc[1]), "+v"(acc[2]), "+v"(pka), "+v"(va), "+v"(csa));
  bar();
  STAMP(2);
#pragma unroll
  for (int i = 0; i < 6; i++) rd(i / 3, i % 3);
#pragma nounroll
  for (int s = 1; s < 8; s++) {
    step(0, true);
    step(1, true);
    sb = sb == 2 * G2_SLOT ? 0 : sb + G2_SLOT;
    bar();
    step(2, true);
    step(3, true);
  }
  step(0, true);
  step(1, true);
  bar();
  __builtin_amdgcn_s_setprio(1);
  step(2, false);
  f32x4 ck[4], cv[4];
#pragma unroll
  for (int i = 0; i < 4; i++) { ck[i] = *(LAS f32x4*)(csa + 128 + 32 * i); cv[i] = *(LAS f32x4*)(csa + 256 + 32 * i); }
  const f32x2 rm = *(LAS f32x2*)(lds + G2_ST + xrow * 8);
  bf16x8 wf[2];
#pragma unroll
  for (int e = 0; e < 2; e++) {
    unsigned a = lds + G2_PHI + (hp * 2 + e) * 512 + (4 * h + q) * 32 + (2 * blk + (pp >> 1)) * 8;
    wf[e] = trd2(a, a + 8 * 32);
  }
  SB0();
  acc[1] = mfma(fw[3][1], fx[3], acc[1]);
  acc[2] = mfma(fw[3][2], fx[3], acc[2]);
  acc[0] = mfma(fw[3][0], fx[3], acc[0]);
  SB0();
  STAMP(3);
#pragma unroll
  for (int e = 0; e < 2; e++) {
    u32x4 u = __builtin_bit_cast(u32x4, wf[e]);
    unsigned sgn = (q & 1) ? 0x80008000u : 0u;
#pragma unroll
    for (int jj = 0; jj < 4; jj++) u[jj] ^= sgn;
    wf[e] = __builtin_bit_cast(bf16x8, u);
  }
  const float r = rm[0], nm = -rm[1], sc = r * 1.4426950408889634f;
  const f32x16 z16 = {};
  {
    unsigned o[8];
    center<false>(acc[1], ck, nm, r, o);
    f32x16 pk0 = mfma(wf[0], frag(o, 0), z16), pk1 = mfma(wf[1], frag(o, 1), z16);
    SB0();
    center<true>(acc[2], cv, nm, r, o);
#pragma unroll
    for (int g = 0; g < 4; g++) *(LAS u32x2*)(va ^ (g * 16)) = u32x2{o[2 * g], o[2 * g + 1]};
    softmax16(pk0, sc, o);
#pragma unroll
    for (int g = 0; g < 4; g++) *(LAS u32x2*)(pka ^ (g * 16)) = u32x2{o[2 * g], o[2 * g + 1]};
    softmax16(pk1, sc, o);
#pragma unroll
    for (int g = 0; g < 4; g++) *(LAS u32x2*)(pka ^ (64 + g * 16)) = u32x2{o[2 * g], o[2 * g + 1]};
  }
  const unsigned pwm = lds + L_PW + w * 4, pwa = lds + L_PW + (l & 7) * 4;
  prog_put(pwm, 1);
  SB0();
  STAMP(5);
  __builtin_amdgcn_s_setprio(0);
  bf16x8 phq[2][2];
  {
    f32x4 cq[4];
#pragma unroll
    for (int i = 0; i < 4; i++) cq[i] = *(LAS f32x4*)(csa + 32 * i);
    unsigned o[8];
    center<false>(acc[0], cq, nm, r, o);
    f32x16 pq0 = mfma(wf[0], frag(o, 0), z16), pq1 = mfma(wf[1], frag(o, 1), z16);
    softmax16(pq0, sc, o);
    phq[0][0] = frag(o, 0); phq[0][1] = frag(o, 1);
    softmax16(pq1, sc, o);
    phq[1][0] = frag(o, 0); phq[1][1] = frag(o, 1);
  }
  const int hh = w & 3, ee = w >> 2, head = hg * 4 + hh;
  unsigned ko = WS_KVP + ((l >> 5) * 32 + head) * 1024 + ee * 512 + (l & 31) * 16, zo = l < 32 ? OOB : WS_ZP + head * 1024 + ee * 512 + (l & 31) * 16;
  unsigned ta = lds + G2_T + hh * 2048 + ee * 1024 + (((l >> 4) & 1) * 32 + (l >> 5) * 16 + (l & 15)) * 16;
  unsigned tr_ = lds + G2_T + hp * 4096 + l * 16, oo = WS_O + l * 16 + ((rt * 4 + mq) * 32 + hg * 4 + hp * 2) * 1024;
  asm volatile("" : "+v"(ko), "+v"(zo), "+v"(ta), "+v"(tr_), "+v"(oo), "+v"(phq[0][0]), "+v"(phq[0][1]), "+v"(phq[1][0]), "+v"(phq[1][1]));
  lds_wait(lds + L_CTL + 12, 1);
  STAMP(8);
  {
    u32x4 v[8];
#pragma unroll
    for (int i = 0; i < 8; i++) v[i] = ld16<SC1>(rw, ko, i * 65536);
    u32x4 zv = ld16<SC1>(rw, zo, 0);
    FSTV(10, zv[0]);
    float s[8] = {};
    unsigned sel_lo = 0x00003f80u;
    asm("" : "+s"(sel_lo));
#pragma unroll
    for (int i = 0; i < 8; i++)
#pragma unroll
      for (int jj = 0; jj < 4; jj++) { s[2 * jj] = dot2(v[i][jj], sel_lo, s[2 * jj]); s[2 * jj + 1] = dot2(v[i][jj], 0x3f800000u, s[2 * jj + 1]); }
#pragma unroll
    for (int i = 0; i < 8; i++) s[i] = half_sum(s[i]);
    u32x4 o = {pk(s[0], s[1]), pk(s[2], s[3]), pk(s[4], s[5]), pk(s[6], s[7])};
    if (l >= 32) o = zv;
    *(LAS u32x4*)ta = o;
  }
  FST(11);
  prog_put(pwm, 2);
  prog_wait(pwa, 2);
  FST(12);
  bf16x8 ta_[2][2];
#pragma unroll
  for (int e = 0; e < 2; e++)
#pragma unroll
    for (int c = 0; c < 2; c++) ta_[e][c] = lds16(tr_ + e * 2048 + c * 1024);
  SB0();
  f32x16 a[2];
#pragma unroll
  for (int e = 0; e < 2; e++) a[e] = mfma(ta_[e][1], phq[e][1], mfma(ta_[e][0], phq[e][0], z16));
  FSTV(13, a[1][0]);
#pragma unroll
  for (int e = 0; e < 2; e++) {
    const f32x2 d2 = (f32x2{a[e][8], a[e][9]} + f32x2{a[e][10], a[e][11]}) + (f32x2{a[e][12], a[e][13]} + f32x2{a[e][14], a[e][15]});
    const float den = __builtin_amdgcn_rcpf(half_sum(d2[0] + d2[1]) + 1e-6f);
    u32x4 o;
#pragma unroll
    for (int i = 0; i < 4; i++) { const f32x2 u = f32x2{a[e][2 * i], a[e][2 * i + 1]} * den; o[i] = pk(u[0], u[1]); }
    st16<SC1>(o, rw, oo + e * 1024);
  }
  STAMP(15);
}

DI void p2_touch(const Params& p, int st, int dt) {
  const int l = threadIdx.x & 63;
  rsrc_t rw2 = mk(p.w2, F * S * 2), rwo = mk(p.wo, D * D * 2);
  unsigned sink = 0;
#pragma nounroll
  for (int k = 0; k < 11; k++) sink += __builtin_amdgcn_raw_buffer_load_b32(rw2, (l >> 3) * 4096 + (l & 7) * 128, (k * 16 + (dt & 1) * 8 + (st & 7)) * (8 * 4096) + (st >> 3) * 1024, 0);
#pragma nounroll
  for (int k = 0; k < 2; k++) sink += __builtin_amdgcn_raw_buffer_load_b32(rwo, (l >> 2) * 1024 + (l & 3) * 128, (k * 16 + (dt & 1) * 8 + (st & 7)) * (16 * 1024) + (dt >> 2) * 512, 0);
  asm volatile("" ::"v"(sink));
}
DI void p2_loader(const Params& p, unsigned lds, int st, int dt, int j, bool g1t) {
  const int l = threadIdx.x & 63;
  rsrc_t rwo = mk(p.wo, D * D * 2), rw = mk(p.ws, WS_BYTES);
  const unsigned tvo = (j * 32 + (l >> 3)) * 1024 + ((l & 7) ^ (((l >> 4) & 1) << 2)) * 16, base = lds + j * 4096;
  const TW tw(p, lds, st, dt, j);
  const int rot = tw.rot;
  tw(6, 6);
  bar();
  tw(7, 7); tw(8, 0); tw(9, 1); tw(10, 2);
  wvb<0>();
  STAMPW(6, j == 0);
  bar();
  STAMPW(7, j == 0);
  auto two = [&](int u) {
    const unsigned to = (((u >> 1) + rot) & 3) * (128 * 1024) + dt * 128 + (u & 1) * (16 * 1024), dst = base + (u >> 1) * P2_S + (u & 1) * 2048;
    dma16(rwo, tvo, to, dst);
    dma16(rwo, tvo, to + 8 * 1024, dst + 1024);
  };
#pragma nounroll
  for (int u = 0; u < 8; u++) two(u);
  const unsigned g0 = WS_G + (dt * 2 + (j >> 1)) * GT * 1024 + (j & 1) * 4096, o0 = WS_O + (st * 2 + (j >> 1)) * 32768 + (j & 1) * 4096;
  unsigned gc = (rot % 11) * 8192, vl = l * 16;
  const unsigned f4 = WS_F4 + ((st >> 1) * 8 + (l & 7)) * 128, tag = lds_tag(lds);
  auto issue = [&](int s) {
    if (s < 11) { dma16x4_sc1(rw, vl, g0 + gc, base + (4 + s % 5) * P2_S); gc = gc == 10 * 8192 ? 0 : gc + 8192; }
    else dma16x4_sc1(rw, vl, o0 + ((s - 11 + rot) & 3) * 8192, base + (4 + s % 5) * P2_S);
  };
  bar();
  STAMPW(10, j == 0);
#pragma unroll
  for (int s = 0; s < L_AHEAD; s++) issue(s);
  u32x2 fo;
  wvb<4 * L_AHEAD - 4>();
#pragma unroll
  for (int s = 0; s < 14; s++) {
    if (s + 2 * L_AHEAD == 11) asm volatile("buffer_load_dwordx2 %0, %1, %2, 0 offen sc1" : "=v"(fo) : "v"(f4), "s"(rw) : "memory");
    if (s + L_AHEAD == 11) {
      asm volatile("" : "+v"(fo));
      if (!__all(fo[0] == tag && fo[1] == ~tag)) poll_flags(rw, f4, tag);
    }
    if (s + L_AHEAD < 15) { issue(s + L_AHEAD); wvb<4 * L_AHEAD - 4>(); }
    else if (13 - s == 2) wvb<8>();
    else if (13 - s == 1) wvb<4>();
    else wvb<0>();
  }
  bar();
  bar();
}
DI void p2_compute(const Params& p, unsigned lds, int st, int dt, int w, unsigned pflag, bool g1t, TP& tp) {
  const int t = threadIdx.x, l = t & 63;
  rsrc_t rx = mk(p.x, S * D * 2), rb2 = mk(p.b2, S * 2), ro = mk(p.out, S * D * 2);
  const unsigned ov = (t >> 3) * 1024 + (t & 7) * 16 + st * 65536 + dt * 128;
  const int h = l >> 5, blk = (l >> 4) & 1, q = (l & 15) >> 2, pp = l & 3;
  const int kq = w >> 1, ms = w & 1;
  const unsigned t0 = p2_t0(lds, w), tm = t0 ^ (ms * 64), t1 = t0 ^ 64;
  rsrc_t rw = mk(p.ws, WS_BYTES);
  drain();
  if (pflag) {
    if (lds_arrive(lds + L_CTL + 56, g1t ? 6 : 8)) {
      if (l == 0) put_flag(rw, pflag, lds_tag(lds));
    }
    STAMP(9);
  }
  u32x4 xres = ld16(rx, ov, 0);
  unsigned short b2v = __builtin_amdgcn_raw_buffer_load_b16(rb2, (t >> 3) * 2, st * 128, 0);
  bar();
  pull(tp, 4, tm + 3 * P2_S); pull(tp, 8, tm); pull(tp, 9, tm + P2_S); pull(tp, 10, tm + 2 * P2_S);
  pull(tp, 5, tm + (g1t ? 8 : 5) * P2_S); pull(tp, 6, tm + 6 * P2_S); pull(tp, 7, tm + 7 * P2_S);
  bar();
  f32x16 acc[2] = {};
  asm volatile("" : "+v"(acc[0]), "+v"(acc[1]));
  if (w == 7) poll_flags(rw, WS_F3 + (dt * 16 + ((l & 15) < 15 ? (l & 15) : 0)) * 128, lds_tag(lds));
  bar();
  const unsigned la = lds + 4 * P2_S + kq * 2048 + l * 16, la4 = la + 4 * P2_S;
  bf16x8 fa[2][2][2], fo[2][2];
  auto rd = [&](int s, int k) {
    const int buf = s & 1, sl = s % 5;
    const unsigned a = (sl < 4 ? la + sl * P2_S : la4) + k * 1024;
    if (s < 11) {
      fa[buf][k][0] = lds16(a);
      fa[buf][k][1] = lds16(a + 8192);
    } else {
      fo[buf][k] = lds16(a + ms * 8192);
      const unsigned o = (s - 11) * P2_S + k * 2048;
      fa[buf][k][0] = trd2(t0 + o, t0 + o + 1024);
      fa[buf][k][1] = trd2(t1 + o, t1 + o + 1024);
    }
  };
  bar();
  STAMP(11);
  rd(0, 0); rd(0, 1);
#pragma unroll
  for (int s = 0; s < 15; s++) {
    bar();
#pragma unroll
    for (int k = 0; k < 2; k++) {
      const bf16x8 bop = s < 11 ? tp.f[s < 11 ? s : 0][k] : fo[s & 1][k];
      acc[0] = mfma(fa[s & 1][k][0], bop, acc[0]);
      SB0();
      if (s < 14 && k == 0) { rd(s + 1, 0); rd(s + 1, 1); }
      SB0();
      acc[1] = mfma(fa[s & 1][k][1], bop, acc[1]);
      SB0();
    }
  }
  STAMP(12);
#pragma unroll
  for (int nd = 0; nd < 2; nd++)
#pragma unroll
    for (int g = 0; g < 4; g++) {
      f32x4 v = {acc[nd][4 * g], acc[nd][4 * g + 1], acc[nd][4 * g + 2], acc[nd][4 * g + 3]};
      *(LAS f32x4*)(lds + kq * 17408 + (ms * 32 + (l & 31)) * 272 + (nd * 32 + 8 * g + 4 * h) * 4) = v;
    }
  bar();
  {
    const unsigned ra = lds + (t >> 3) * 272 + (t & 7) * 32;
    f32x4 a0 = (*(LAS f32x4*)ra + *(LAS f32x4*)(ra + 17408)) + (*(LAS f32x4*)(ra + 2 * 17408) + *(LAS f32x4*)(ra + 3 * 17408));
    f32x4 a1 = (*(LAS f32x4*)(ra + 16) + *(LAS f32x4*)(ra + 17408 + 16)) + (*(LAS f32x4*)(ra + 2 * 17408 + 16) + *(LAS f32x4*)(ra + 3 * 17408 + 16));
    const float bb = blo(b2v);
    u32x4 o = {pk(a0[0] + bb + blo(xres[0]), a0[1] + bb + bhi(xres[0])), pk(a0[2] + bb + blo(xres[1]), a0[3] + bb + bhi(xres[1])),
               pk(a1[0] + bb + blo(xres[2]), a1[1] + bb + bhi(xres[2])), pk(a1[2] + bb + blo(xres[3]), a1[3] + bb + bhi(xres[3]))};
    st16(o, ro, ov);
  }
  STAMP(13);
}

extern __shared__ __attribute__((aligned(128))) char smem[];
DI unsigned lds_base() { return (unsigned)(size_t)(LAS char*)smem; }

__global__ __launch_bounds__(NT, 3) void k_main(Params p) {
  const int b = blockIdx.x, xr = (b + XROT) & 7, i = b >> 3, l = threadIdx.x & 63, w = __builtin_amdgcn_readfirstlane(threadIdx.x >> 6);
  const unsigned lds = lds_base();
  STAMP(0);
  rsrc_t rw = mk(p.ws, WS_BYTES);
  const int st = (xr >> 1) * 8 + (i & 7), dt2 = (xr & 1) * 4 + (i >> 3);
  const bool g1 = i < 16;
  const int gx = xr & 3;
  const bool crew = g1 && (w < 6 || w >= 10);
  const int c = w < 6 ? w : w - 4, lb = xr * 16 + (i & 15);
  u32x4 v0, v1;
  if (crew) {
    if (CPRIO) __builtin_amdgcn_s_setprio(CPRIO);
    rsrc_t rx = mk(p.x, S * D * 2);
    const unsigned o = (lb * 16 + c * 2 + (l >> 5)) * 1024 + (l & 31) * 16;
    v0 = ld16(rx, o, 0);
    v1 = ld16(rx, o + 512, 0);
  }
  unsigned seedv = 0;
  if (crew || w == 0) seedv = __builtin_amdgcn_raw_buffer_load_b32(rw, WS_SEED, 0, SC1);
  if (threadIdx.x < 128) *(LAS unsigned*)(lds + L_CTL + threadIdx.x * 4) = 0u;
  bar();
  if (w == 0) { *(volatile LAS unsigned*)(lds + L_CTL + 24) = seedv + 1u; lds_set(lds + L_CTL + 52); }
  unsigned pflag = 0;
  bool g1t = false, pulled = false;
  TP tp;
  if (g1) {
    const int dt = (xr & 1) * 4 + (i & 3), ft = (xr >> 1) * 4 + (i >> 2);
    const int rot = (4 * (i & 3) + 5 * (i >> 2) + 2 * xr) & 15;
    const bool bumper = xr == 6 && i == 12;
    if (!crew) {
      if (ft >= 14) cs_phase(p, lds, (ft - 14) * 8 + dt, w - 6, 0);
      if (ft == 15) cs_phase(p, lds, 16 + dt, w - 6, 1);
      if (ft < 15) g1_wloader(p, lds, ft, rot, w - 6, st, dt2);
    } else {
      const unsigned tag = __builtin_amdgcn_readfirstlane(seedv) + 1u;
      ln_phase(p, lds, lb, c, tag, v0, v1);
      STAMP(1);
      if (ft < 15) {
        if (w < 6) { __builtin_amdgcn_s_setprio(0); if (G1PRIO == 1 && w >= 4) __builtin_amdgcn_s_setprio(1); if (G1PRIO == 2) __builtin_amdgcn_s_setprio(1); g1_compute(p, lds, dt, ft, w, tp); pulled = true; pflag = WS_F3 + (dt * 16 + ft) * 128; STAMP(5); }
        else { g1_xloader(p, lds, dt, rot, w - 10, tag); bar(); }
      } else if (bumper && w == 0) {
        poll_ln(rw, tag);
        poll_flags(rw, WS_F2 + l * 128, tag);
        poll_flags(rw, WS_F2 + 8192 + l * 128, tag);
        if (l == 0) __builtin_amdgcn_raw_buffer_store_b32(tag, rw, WS_SEED, 0, SC1);
      }
    }
    g1t = ft < 15;
    if (!g1t) {
      bar();
      if (w >= 8) {
        const TW tw(p, lds, st, dt2, w - 8);
#pragma nounroll
        for (int s = 0; s < 6; s++) tw(s, s == 3 ? 4 : s == 4 ? 3 : s);
      }
      wvb<0>();
    }
  } else {
    const int rt = (xr >> 1) * 4 + (i & 3), hg = (xr & 1) * 4 + ((i >> 2) & 3), rot = G2ROT == 0 ? xr : G2ROT == 1 ? (xr + ((i + (i >> 2)) & 1)) & 7 : ((i & 3) * 2 + ((i >> 2) & 3) * 3) & 7;
    if (w >= 8) {
      g2_loader(p, lds, rt, hg, rot, w - 8);
      g2_kv(p, lds, rt, hg, w - 8);
      if (w == 11 && T_GATE) {
        lds_wait(lds + L_CTL, 4);
        __builtin_amdgcn_s_sleep(LN_DELAY);
        poll_flags(rw, WS_F2 + (hg * 16 + (l & 15)) * 128, lds_tag(lds));
        lds_set(lds + L_CTL + 12);
      }
      if (T_GATE) { lds_wait(lds + L_CTL + 12, 1); prog_wait(lds + L_PW + (l & 7) * 4, 2); }
      const TW tw(p, lds, st, dt2, w - 8);
#pragma nounroll
      for (int s = 0; s < 6; s++) { tw(s, s == 3 ? 4 : s == 4 ? 3 : s); wv<T_PACE>(); if (T_SLEEP) __builtin_amdgcn_s_sleep(T_SLEEP); }
      STAMPW(1, w == 8);
      if (w == 11 && !T_GATE) {
        lds_wait(lds + L_CTL, 4);
        __builtin_amdgcn_s_sleep(LN_DELAY);
        poll_flags(rw, WS_F2 + (hg * 16 + (l & 15)) * 128, lds_tag(lds));
        lds_set(lds + L_CTL + 12);
      }
    } else {
      g2_compute(p, lds, rt, hg, w);
      pflag = WS_F4 + (rt * 8 + hg) * 128;
    }
    if (w >= 8) wvb<0>(); else bar();
  }
  if (w < 8) {
    if (!pulled) {
      pull03(tp, p2_t0(lds, w) ^ ((w & 1) * 64), g1t);
      bar();
    }
    p2_compute(p, lds, st, dt2, w, pflag, g1t, tp);
  }
  else p2_loader(p, lds, st, dt2, w - 8, g1t);
}
}

extern "C" void kernel_launch(void* const* d_in, const int* in_sizes, int n_in, void* d_out, int out_size, void* d_ws, size_t ws_size,
                              hipStream_t stream) {
  using namespace hk;
  Params p{d_in[0], d_in[1], d_in[2], d_in[3], d_in[5], d_in[6], d_in[7], d_in[8], d_out, d_ws};
  (void)hipFuncSetAttribute((const void*)k_main, hipFuncAttributeMaxDynamicSharedMemorySize, LDS_BYTES);
  k_main<<<256, NT, LDS_BYTES, stream>>>(p);
}
```

```cpp
#include <hip/hip_runtime.h>
#include <hip/hip_bf16.h>
#define FUSE_P2 1
#define MIXED 1
#define W_EARLY 0
#define SPIN_SLEEP 1
#define POLL_SLEEP 1
#define LPRIO 0
#define WO_EARLY 1
#define LN_DELAY 12
#define KV_DELAY 16
#define G1PRIO 0
#define XROT 7
#define L_AHEAD 4
#define G1BAL 0
#define T_EARLY 0
#define KV_CPOLL 1
#define TOUCH 1
#define OUT_AUX 0
#define LOOK2 0
#define G2NT 0
#define GROT8
#define G2ROT 2
#define CPRIO 3
#define T_PACE 40
#define T_SLEEP 0
#define T_GATE 0
#define W_GATE 12
#define CODE_BYTES 0

namespace hk {
#define DI __device__ __forceinline__
#define LAS __attribute__((address_space(3)))
typedef short bf16x8 __attribute__((ext_vector_type(8)));
typedef short s16x4 __attribute__((ext_vector_type(4)));
typedef float f32x16 __attribute__((ext_vector_type(16)));
typedef float f32x4 __attribute__((ext_vector_type(4)));
typedef float f32x2 __attribute__((ext_vector_type(2)));
typedef unsigned u32x4 __attribute__((ext_vector_type(4)));
typedef unsigned u32x2 __attribute__((ext_vector_type(2)));
typedef __bf16 bfx2 __attribute__((ext_vector_type(2)));
typedef __amdgpu_buffer_rsrc_t rsrc_t;

constexpr int S = 2048, D = 512, F = 1376;
constexpr int GT = 88;
constexpr int WS_SEED = 0;
constexpr int WS_F2 = 4096;
constexpr int WS_F3 = 20480;
constexpr int WS_F4 = 36864;
constexpr int WS_CS = 53248;
constexpr int WS_SB = 65536;
constexpr int WS_ZP = 98304;
constexpr int WS_KVP = 131072;
constexpr int WS_G = 1 << 20;
constexpr int WS_O = 3 << 20;
constexpr int WS_H = 5 << 20;
constexpr int WS_BYTES = 7 << 20;
constexpr int SC1 = 16;
constexpr int NT = 768;
constexpr unsigned OOB = 0x40000000u;
constexpr int SPIN_CAP = 1 << 14;

#define STAMP(i)
#define STAMPW(i, cond)
#define FST(i)
#define FSTW(i)
#define FSTV(i, v)
struct Params {
  const void *x, *wqkv, *wphi, *wo, *w1, *b1, *w2, *b2;
  void *out, *ws;
};

#define SB0() __builtin_amdgcn_sched_barrier(0)
DI rsrc_t mk(const void* p, int bytes) { return __builtin_amdgcn_make_buffer_rsrc((void*)p, 0, bytes, 0x00020000); }
DI void dma16(rsrc_t r, unsigned voff, unsigned soff, unsigned lds) {
  asm volatile("s_mov_b32 m0, %0\n\ts_nop 0\n\tbuffer_load_dwordx4 %1, %2, %3 offen lds" ::"s"(lds), "v"(voff), "s"(r), "s"(soff) : "memory");
}
DI void dma16_nt(rsrc_t r, unsigned voff, unsigned soff, unsigned lds) {
  asm volatile("s_mov_b32 m0, %0\n\ts_nop 0\n\tbuffer_load_dwordx4 %1, %2, %3 offen nt lds" ::"s"(lds), "v"(voff), "s"(r), "s"(soff) : "memory");
}
#define DMAX dma16
#define DMAQ dma16
DI void dma16_sc1(rsrc_t r, unsigned voff, unsigned soff, unsigned lds) {
  asm volatile("s_mov_b32 m0, %0\n\ts_nop 0\n\tbuffer_load_dwordx4 %1, %2, %3 offen sc1 lds" ::"s"(lds), "v"(voff), "s"(r), "s"(soff) : "memory");
}
DI void dma16x4_sc1(rsrc_t r, unsigned voff, unsigned soff, unsigned lds) {
  asm volatile("s_mov_b32 m0, %0\n\ts_nop 0\n\tbuffer_load_dwordx4 %1, %2, %3 offen sc1 lds\n\tbuffer_load_dwordx4 %1, %2, %3 offen offset:1024 sc1 lds\n\t"
               "buffer_load_dwordx4 %1, %2, %3 offen offset:2048 sc1 lds\n\tbuffer_load_dwordx4 %1, %2, %3 offen offset:3072 sc1 lds" ::"s"(lds), "v"(voff), "s"(r), "s"(soff) : "memory");
}
DI unsigned lds_tag(unsigned lds);
DI void lds_set(unsigned a) { *(volatile LAS unsigned*)a = 1u; }
template <int N> DI void wvb() { asm volatile("s_waitcnt vmcnt(%0) lgkmcnt(0)\n\ts_barrier" ::"i"(N) : "memory"); }
DI void bar() { asm volatile("s_waitcnt lgkmcnt(0)\n\ts_barrier" ::: "memory"); }
DI void drain() { asm volatile("s_waitcnt vmcnt(0)" ::: "memory"); }
template <int AUX = 0> DI u32x4 ld16(rsrc_t r, unsigned voff, unsigned soff) { return __builtin_amdgcn_raw_buffer_load_b128(r, voff, soff, AUX); }
template <int AUX = 0> DI u32x2 ld8(rsrc_t r, unsigned voff, unsigned soff) { return __builtin_amdgcn_raw_buffer_load_b64(r, voff, soff, AUX); }
template <int AUX = 0> DI void st16(u32x4 v, rsrc_t r, unsigned voff) { __builtin_amdgcn_raw_buffer_store_b128(v, r, voff, 0, AUX); }
DI s16x4 trd(unsigned a) { return __builtin_amdgcn_ds_read_tr16_b64_v4i16((LAS s16x4*)a); }
DI bf16x8 trd2(unsigned a0, unsigned a1) {
  s16x4 lo = trd(a0), hi = trd(a1);
  return __builtin_shufflevector(lo, hi, 0, 1, 2, 3, 4, 5, 6, 7);
}
DI bf16x8 lds16(unsigned a) { return *(LAS bf16x8*)a; }
DI f32x16 mfma(bf16x8 a, bf16x8 b, f32x16 c) { return
 __builtin_amdgcn_mfma_f32_32x32x16_bf16(a, b, c, 0, 0, 0); }
DI unsigned pk(float lo, float hi) {
  f32x2 v = {lo, hi};
  return __builtin_bit_cast(unsigned, __builtin_convertvector(v, bfx2));
}
DI float asf(unsigned u) { return __builtin_bit_cast(float, u); }
DI unsigned asu(float u) { return __builtin_bit_cast(unsigned, u); }
DI float blo(unsigned u) { return asf(u << 16); }
DI float bhi(unsigned u) { return asf(u & 0xffff0000u); }
DI u32x4 pack8u(const f32x16& x, int s) {
  u32x4 p = {pk(x[8 * s], x[8 * s + 1]), pk(x[8 * s + 2], x[8 * s + 3]), pk(x[8 * s + 4], x[8 * s + 5]), pk(x[8 * s + 6], x[8 * s + 7])};
  return p;
}
DI bf16x8 pack8(const f32x16& x, int s) { return __builtin_bit_cast(bf16x8, pack8u(x, s)); }
DI float dot2(unsigned a, unsigned b, float c) { return __builtin_amdgcn_fdot2_f32_bf16(__builtin_bit_cast(bfx2, a), __builtin_bit_cast(bfx2, b), c, false); }
DI float half_sum(float v) {
  unsigned u = asu(v);
  auto r = __builtin_amdgcn_permlane32_swap(u, u, false, false);
  return asf(r[0]) + asf(r[1]);
}
template <int C> DI float dpp_add(float v) { return v + __builtin_amdgcn_update_dpp(0.f, v, C, 0xf, 0xf, false); }
DI float half32_sum(float v) {
  v = dpp_add<0x128>(v);
  v = dpp_add<0x124>(v);
  v = dpp_add<0x122>(v);
  v = dpp_add<0x121>(v);
  unsigned u = asu(v);
  auto r = __builtin_amdgcn_permlane16_swap(u, u, false, false);
  return asf(r[0]) + asf(r[1]);
}
DI void put_flag(rsrc_t rw, unsigned off, unsigned tag) {
  u32x2 f = {tag, ~tag};
  __builtin_amdgcn_raw_buffer_store_b64(f, rw, off, 0, SC1);
}
DI void poll_flags(rsrc_t rw, unsigned off, unsigned tag) {
#pragma nounroll
  for (int sp = 0; sp < SPIN_CAP; sp++) {
    asm volatile("" ::: "memory");
    u32x2 f = ld8<SC1>(rw, off, 0);
    if (__all(f[0] == tag && f[1] == ~tag)) break;
    __builtin_amdgcn_s_sleep(1);
  }
  asm volatile("" ::: "memory");
}
DI bool lds_arrive(unsigned cnt, unsigned n) {
  asm volatile("s_waitcnt lgkmcnt(0)" ::: "memory");
  unsigned old = 0;
  if ((threadIdx.x & 63) == 0) old = __hip_atomic_fetch_add((LAS unsigned*)cnt, 1u, __ATOMIC_RELAXED, __HIP_MEMORY_SCOPE_WORKGROUP);
  old = __builtin_amdgcn_readfirstlane(old);
  asm volatile("" ::: "memory");
  return old == n - 1;
}
DI void lds_wait(unsigned cnt, unsigned n) {
  while (__hip_atomic_load((LAS unsigned*)cnt, __ATOMIC_RELAXED, __HIP_MEMORY_SCOPE_WORKGROUP) < n) __builtin_amdgcn_s_sleep(SPIN_SLEEP);
  asm volatile("" ::: "memory");
}

DI void prog_put(unsigned a, unsigned v) { asm volatile("" ::: "memory"); *(volatile LAS unsigned*)a = v; asm volatile("" ::: "memory"); }
DI void prog_wait(unsigned a, unsigned n) {
  unsigned seen;
  asm volatile(
      ".Lpw_loop%=:\n\tds_read_b32 %0, %1\n\ts_waitcnt lgkmcnt(0)\n\t"
      "v_cmp_le_u32 vcc, %2, %0\n\ts_cmp_eq_u64 vcc, exec\n\ts_cbranch_scc0 .Lpw_loop%="
      : "=&v"(seen) : "v"(a), "s"(n) : "vcc", "scc", "memory");
}
constexpr int L_CTL = 147456;
constexpr int L_PW = L_CTL + 320;
constexpr int L_MISC = L_CTL + 512;
constexpr int LDS_BYTES = L_MISC + 9216;
constexpr int G1_W = 0, G1_WS = 24576, G1_X = 4 * G1_WS, G1_XS = 16384;
constexpr int G2_SLOT = 40960, G2_XO = 0, G2_WO = 16384, G2_PHI = L_MISC, G2_ONES = G2_PHI + 2048, G2_CS = G2_ONES + 64, G2_ST = G2_CS + 768;
constexpr int G2_PK = 98304, G2_V = G2_PK + 32768, G2_T = G2_V;
constexpr int P2_T = 0, P2_L = 98304, P2_S = 16384;
static_assert(G2_ST + 1024 <= LDS_BYTES && LDS_BYTES <= 163840, "lds");

DI unsigned lds_tag(unsigned lds) { return __builtin_amdgcn_readfirstlane(*(volatile LAS unsigned*)(lds + L_CTL + 24)); }

DI void ln_phase(const Params& p, unsigned lds, int lb, int c, unsigned tag, u32x4 v0, u32x4 v1) {
  const int l = threadIdx.x & 63;
  rsrc_t rw = mk(p.ws, WS_BYTES);
  float s = 0.f, s1 = 0.f;
#pragma unroll
  for (int i = 0; i < 4; i++) { s = dot2(v0[i], 0x3f803f80u, s); s1 = dot2(v1[i], 0x3f803f80u, s1); }
  const float m = half32_sum(s + s1) * (1.0f / D);
  f32x2 e[8], se = {0.f, 0.f}, sq = {0.f, 0.f};
#pragma unroll
  for (int i = 0; i < 4; i++) {
    e[i] = f32x2{blo(v0[i]), bhi(v0[i])} - m;
    e[4 + i] = f32x2{blo(v1[i]), bhi(v1[i])} - m;
  }
#pragma unroll
  for (int i = 0; i < 8; i++) { se += e[i]; sq += e[i] * e[i]; }
  const float me = half32_sum(se[0] + se[1]) * (1.0f / D), var = half32_sum(sq[0] + sq[1]) * (1.0f / D);
  const float r = __builtin_amdgcn_rsqf(var + 1e-5f);
  const float m2 = me * r, v2 = fmaxf(var - me * me, 0.f) * r * r;
  const float r2 = __builtin_amdgcn_rsqf(v2 + 1e-5f);
  const float ca = r * r2, cb = -m2 * r2;
  u32x4 o0, o1;
#pragma unroll
  for (int i = 0; i < 4; i++) {
    f32x2 u = e[i] * ca + cb, u2 = e[4 + i] * ca + cb;
    o0[i] = pk(u[0], u[1]);
    o1[i] = pk(u2[0], u2[1]);
  }
  const int row = c * 2 + (l >> 5);
  const unsigned ho = WS_H + ((l & 31) >> 3) * (S * 128) + (lb * 16 + row) * 128 + (((l & 7) ^ ((c & 1) << 2)) * 16);
  st16<SC1>(o0, rw, ho);
  st16<SC1>(o1, rw, ho + 4 * (S * 128));
  if ((l & 31) == 0) {
    u32x4 g = {asu(r), tag, asu(m), tag};
    *(LAS u32x4*)(lds + L_CTL + 64 + row * 16) = g;
  }
  if (W_GATE == 0) lds_arrive(lds + L_CTL, 8);
  drain();
  if (lds_arrive(lds + L_CTL + 4, 8)) {
    if (l < 16) {
      u32x4 g = *(LAS u32x4*)(lds + L_CTL + 64 + l * 16);
      st16<SC1>(g, rw, WS_SB + lb * 256 + l * 16);
    }
    lds_set(lds + L_CTL + 12);
    STAMPW(8, true);
    STAMPW(15, true);
  }
}
DI void poll_ln(rsrc_t rw, unsigned tag) {
  const int l = threadIdx.x & 63;
  bool ok0 = false, ok1 = false;
#pragma nounroll
  for (int sp = 0; sp < SPIN_CAP; sp++) {
    asm volatile("" ::: "memory");
    u32x4 f0 = ld16<SC1>(rw, ok0 ? OOB : WS_SB + l * 256, 0), f1 = ld16<SC1>(rw, ok1 ? OOB : WS_SB + 16384 + l * 256, 0);
    ok0 = ok0 || (f0[1] == tag && f0[3] == tag);
    ok1 = ok1 || (f1[1] == tag && f1[3] == tag);
    if (__all(ok0 && ok1)) break;
    __builtin_amdgcn_s_sleep(POLL_SLEEP);
  }
  asm volatile("" ::: "memory");
}

DI int g1_stage(int s, int rot) { return ((s + (rot & 8)) & 8) | ((s + rot) & 7); }
DI int p2_rot(int st, int dt) { return (((st & 7) * 11) >> 3) + 6 * (dt & 1) + 3 * ((dt >> 1) & 1); }
struct TW {
  rsrc_t rw2;
  unsigned tv2, base;
  int rot, st;
  DI TW(const Params& p, unsigned lds, int st_, int dt, int j) {
    const int l = threadIdx.x & 63;
    rw2 = mk(p.w2, F * S * 2);
    tv2 = (j * 32 + (l >> 3)) * 4096 + ((l & 7) ^ (((l >> 4) & 1) << 2)) * 16;
    base = lds + j * 4096; rot = p2_rot(st_, dt); st = st_;
  }
  DI void operator()(int s, int slot) const {
    const unsigned to = ((s + rot) % 11) * (128 * 4096) + st * 128;
#pragma unroll
    for (int i = 0; i < 4; i++) dma16(rw2, tv2, to + i * (8 * 4096), base + slot * P2_S + i * 1024);
  }
};
struct TP {
  bf16x8 f[11][2];
};
DI unsigned p2_t0(unsigned lds, int w) {
  const int l = threadIdx.x & 63, h = l >> 5, blk = (l >> 4) & 1, q = (l & 15) >> 2, pp = l & 3;
  return lds + ((w >> 1) * 32 + 4 * h + q) * 128 + (((blk * 2 + (pp >> 1)) ^ (((q >> 1) & 1) << 2)) * 16) + (pp & 1) * 8;
}
DI void pull(TP& t, int s, unsigned a) {
  t.f[s][0] = trd2(a, a + 1024);
  t.f[s][1] = trd2(a + 2048, a + 3072);
}
DI void pull03(TP& t, unsigned tm, bool g1t) {
  pull(t, 0, tm); pull(t, 1, tm + P2_S); pull(t, 2, tm + 2 * P2_S); pull(t, 3, tm + (g1t ? 7 : 4) * P2_S);
}
template <int N> DI void wv() { asm volatile("s_waitcnt vmcnt(%0)" ::"i"(N) : "memory"); }
DI void g1_wloader(const Params& p, unsigned lds, int ft, int rot, int j, int st, int dt2) {
  const int l = threadIdx.x & 63;
  rsrc_t rw1 = mk(p.w1, S * F * 2);
  unsigned wvo[3];
#pragma unroll
  for (int i = 0; i < 3; i++) {
    unsigned u = i * 64 + l;
    unsigned row = u / 12, ch = u - row * 12;
    wvo[i] = (ft * 96 + ch * 8 >= F) ? OOB : (j * 32 + row) * (F * 2) + ch * 16;
  }
  const unsigned wl = lds + G1_W + j * 6144;
  auto issue = [&](int q) {
    const unsigned so = g1_stage(q, rot) * (128 * F * 2) + ft * 192, dst = wl + (q & 3) * G1_WS;
#pragma unroll
    for (int k = 0; k < 6; k++) dma16(rw1, wvo[k % 3], so + (k / 3) * (16 * F * 2), dst + k * 1024);
  };
#pragma nounroll
  for (int q = 0; q < 4; q++) {
    if (q == W_EARLY) lds_wait(lds + L_CTL + W_GATE, W_GATE ? 1 : 8);
    issue(q);
  }
  wvb<18>();
  wvb<12>();
#pragma nounroll
  for (int q = 1; q < 13; q++) {
    issue(q + 3);
    wvb<12>();
  }
  const TW tw(p, lds, st, dt2, j);
  tw(0, 0);
  wvb<10>();
  tw(1, 1); tw(2, 2); tw(3, 7);
  wvb<16>();
  tw(4, 3); tw(5, 8);
  wvb<8>();
}
DI void g1_xloader(const Params& p, unsigned lds, int dt, int rot, int j, unsigned tag) {
  const int l = threadIdx.x & 63;
  rsrc_t rw = mk(p.ws, WS_BYTES);
  auto issue = [&](int n, int slot) {
    const unsigned so = WS_H + dt * (S * 128) + g1_stage(n, rot) * 16384 + j * 8192, xd = lds + G1_X + slot * G1_XS + j * 8192;
    dma16x4_sc1(rw, l * 16, so, xd);
    dma16x4_sc1(rw, l * 16, so + 4096, xd + 4096);
  };
  if (j == 0) { lds_wait(lds + L_CTL + 12, 1);
__builtin_amdgcn_s_sleep(LN_DELAY); poll_ln(rw, tag);
 lds_set(lds + L_CTL + 16); } else lds_wait(lds + L_CTL + 16, 1);
  STAMPW(2, j == 0);
  issue(0, 0); issue(1, 1);
  wvb<8>();
  int slot = 2;
#pragma nounroll
  for (int n = 0; n < 14; n++) {
    issue(n + 2, slot);
    slot = slot == 2 ? 0 : slot + 1;
    wvb<8>();
  }
  wvb<0>();
}
template <int NP> DI void g1_loop(unsigned a0, unsigned b0, f32x16 (&acc)[2], bf16x8 (&fa)[4], bf16x8 (&fx)[4][2]) {
  unsigned aa = a0, ba = b0, bb = b0 ^ 64, xs = 0;
  auto rda = [&](int k, int ks) { fa[k] = trd2(aa + ks * 3072, aa + ks * 3072 + 768); fx[k][0] = trd2(ba + ks * 2048, ba + ks * 2048 + 512); };
  auto rdb = [&](int k, int ks) { fx[k][1] = trd2(bb + ks * 2048, bb + ks * 2048 + 512); };
  auto pair = [&](int par, int nx) {
#pragma unroll
    for (int u = 0; u < 2; u++) {
      const int k = 2 * par + u;
      acc[0] = mfma(fa[k], fx[k][0], acc[0]);
      SB0();
      rda(k ^ 2, 2 * nx + u);
      SB0();
      acc[1] = mfma(fa[k], fx[k][1], acc[1]);
      SB0();
      rdb(k ^ 2, 2 * nx + u);
      SB0();
    }
  };
  auto head = [&](int par) {
#pragma unroll
    for (int i = 0; i + 1 < NP; i++) pair((par + i) & 1, i + 1);
  };
  auto body = [&](int par, int n) {
    head(par);
    xs = xs == 2 * G1_XS ? 0 : xs + G1_XS;
    aa = a0 + (n & 3) * G1_WS; ba = b0 + xs; bb = ba ^ 64;
    bar();
    pair((par + NP - 1) & 1, 0);
  };
  rda(0, 0); rdb(0, 0); rda(1, 1); rdb(1, 1);
#pragma nounroll
  for (int n = 1; n < 15; n += 2) { body(0, n); body(NP & 1, n + 1); }
  body(0, 15);
  head(NP & 1);
  bar();
}
DI void g1_compute(const Params& p, unsigned lds, int dt, int ft, int c, TP& tp) {
  const int l = threadIdx.x & 63;
  rsrc_t rw = mk(p.ws, WS_BYTES), rb1 = mk(p.b1, F * 2);
  const int h = l >> 5, blk = (l >> 4) & 1, q = (l & 15) >> 2, pp = l & 3;
  const int nf = c % 3, kh = c / 3, k0 = kh * 4, pc = kh ? nf : nf + 3;
  const int fb = ft * 96 + nf * 32;
  const unsigned a0 = lds + G1_W + (k0 * 16 + 8 * h + q) * 192 + nf * 64 + blk * 32 + pp * 8;
  const unsigned b0 = (lds + G1_X + (k0 * 16 + 8 * h + q) * 128 + (((blk * 2 + (pp >> 1)) ^ (((q >> 1) & 1) << 2)) * 16) + (pp & 1) * 8) ^ (kh * 64);
  u32x2 bv[4];
#pragma unroll
  for (int g = 0; g < 4; g++) bv[g] = ld8(rb1, (8 * g + 4 * h + fb) * 2, 0);
  f32x16 acc[2] = {};
#pragma unroll
  for (int g = 0; g < 4; g++) { acc[0][4 * g] = blo(bv[g][0]); acc[0][4 * g + 1] = bhi(bv[g][0]); acc[0][4 * g + 2] = blo(bv[g][1]); acc[0][4 * g + 3] = bhi(bv[g][1]); }
  bf16x8 fa[4], fx[4][2];
  unsigned exw = lds + 3 * G1_WS + l * 16 + c * 4096, exr = lds + 3 * G1_WS + l * 16 + pc * 4096;
  unsigned so = fb < GT * 16 ? WS_G + ((dt * 2 + kh) * GT + ft * 6 + nf * 2) * 1024 + l * 16 : OOB;
  unsigned tm = p2_t0(lds, c) ^ ((c & 1) * 64);
  asm volatile("" : "+v"(tm));
  asm volatile("" : "+v"(exw), "+v"(exr), "+v"(so), "+v"(acc[0]), "+v"(acc[1]));
  bar();
  STAMP(3);
  g1_loop<2>(a0, b0, acc, fa, fx);
  acc[1] = mfma(fa[2], fx[2][1], acc[1]);
  SB0();
  pull03(tp, tm, true);
  SB0();
  acc[1] = mfma(fa[3], fx[3][1], acc[1]);
  acc[0] = mfma(fa[2], fx[2][0], acc[0]);
  acc[0] = mfma(fa[3], fx[3][0], acc[0]);
  SB0();
  STAMP(4);
#pragma unroll
  for (int g = 0; g < 4; g++) {
    f32x4 v = {acc[1][4 * g], acc[1][4 * g + 1], acc[1][4 * g + 2], acc[1][4 * g + 3]};
    *(LAS f32x4*)(exw + g * 1024) = v;
  }
  bar();
  u32x4 o[2];
#pragma unroll
  for (int g = 0; g < 4; g++) {
    const f32x4 v = *(LAS f32x4*)(exr + g * 1024);
#pragma unroll
    for (int jj = 0; jj < 2; jj++) {
      const f32x2 u = f32x2{acc[0][4 * g + 2 * jj], acc[0][4 * g + 2 * jj + 1]} + f32x2{v[2 * jj], v[2 * jj + 1]};
      const f32x2 z = u * (u * u * -0.1029432397f + -2.302208198f);
      const f32x2 d = f32x2{__builtin_amdgcn_exp2f(z[0]), __builtin_amdgcn_exp2f(z[1])} + 1.0f;
      const f32x2 r = u * f32x2{__builtin_amdgcn_rcpf(d[0]), __builtin_amdgcn_rcpf(d[1])};
      o[g >> 1][(g & 1) * 2 + jj] = pk(r[0], r[1]);
    }
  }
  st16<SC1>(o[0], rw, so);
  st16<SC1>(o[1], rw, so + 1024);
}

DI float max3(float a, float b, float c) { float r; asm("v_max3_f32 %0, %1, %2, %3" : "=v"(r) : "v"(a), "v"(b), "v"(c)); return r; }
DI void softmax16(f32x16 p, float sc, unsigned (&o)[8]) {
  asm volatile("s_nop 11" : "+v"(p));
  float m0 = max3(p[0], p[1], p[2]), m1 = max3(p[3], p[4], p[5]), m2 = max3(p[6], p[7], p[8]), m3 = max3(p[9], p[10], p[11]), m4 = max3(p[12], p[13], p[14]);
  const float nmx = -max3(max3(m0, m1, p[15]), max3(m2, m3, m4), m0) * sc;
  f32x2 e[8];
#pragma unroll
  for (int i = 0; i < 8; i++) {
    const f32x2 z = f32x2{p[2 * i], p[2 * i + 1]} * sc + nmx;
    e[i] = f32x2{__builtin_amdgcn_exp2f(z[0]), __builtin_amdgcn_exp2f(z[1])};
  }
  const f32x2 s2 = ((e[0] + e[1]) + (e[2] + e[3])) + ((e[4] + e[5]) + (e[6] + e[7]));
  const float s = __builtin_amdgcn_rcpf(s2[0] + s2[1]);
#pragma unroll
  for (int i = 0; i < 8; i++) { const f32x2 r = e[i] * s; o[i] = pk(r[0], r[1]); }
}
template <bool SC> DI void center(const f32x16& a, const f32x4 (&c)[4], float nm, float r, unsigned (&o)[8]) {
#pragma unroll
  for (int i = 0; i < 8; i++) {
    f32x2 v = f32x2{c[i >> 1][(i & 1) * 2], c[i >> 1][(i & 1) * 2 + 1]} * nm + f32x2{a[2 * i], a[2 * i + 1]};
    if (SC) v = v * r;
    o[i] = pk(v[0], v[1]);
  }
}
DI bf16x8 frag(const unsigned (&o)[8], int s) { u32x4 u = {o[4 * s], o[4 * s + 1], o[4 * s + 2], o[4 * s + 3]}; return __builtin_bit_cast(bf16x8, u); }

DI void g2_loader(const Params& p, unsigned lds, int rt, int hg, int rot, int j) {
  const int l = threadIdx.x & 63;
  rsrc_t rx = mk(p.x, S * D * 2), rq = mk(p.wqkv, D * 3 * D * 2), rp = mk(p.wphi, 32 * 256 * 2);
  unsigned xv[2];
#pragma unroll
  for (int i = 0; i < 2; i++) xv[i] = (j * 32 + i * 8 + (l >> 3)) * 1024 + (((l & 7) ^ ((4 * i + (l >> 4)) & 7)) * 16);
  const unsigned wv = (j * 16 + (l >> 3)) * 3072 + (((l & 7) ^ (((l >> 4) & 1) << 2)) * 16);
  auto issue = [&](int s, int slot) {
    const unsigned sidx = (s + rot) & 7, base = lds + slot * G2_SLOT;
    const unsigned xo = rt * (128 * 1024) + sidx * 128, wo = sidx * (64 * 3072) + hg * 128;
#pragma unroll
    for (int i = 0; i < 2; i++) {
      DMAX(rx, xv[0], xo + i * 16384, base + G2_XO + j * 4096 + i * 2048);
      DMAX(rx, xv[1], xo + i * 16384, base + G2_XO + j * 4096 + i * 2048 + 1024);
#pragma unroll
      for (int c = 0; c < 3; c++) DMAQ(rq, wv, wo + i * (8 * 3072) + c * 1024, base + G2_WO + c * 8192 + j * 2048 + i * 1024);
    }
  };
  issue(0, 0);
  issue(1, 1);
  wvb<10>();
  int slot = 2;
  for (int s = 0; s < 6; s++) {
    issue(s + 2, slot);
    slot = slot == 2 ? 0 : slot + 1;
    wvb<10>();
  }
  if (j < 2) dma16(rp, l * 16 + j * 1024, hg * 2048, lds + G2_PHI + j * 1024);
  {
    rsrc_t rw = mk(p.ws, WS_BYTES);
    const unsigned tag = lds_tag(lds);
    const unsigned off = j < 2 ? WS_SB + (rt * 128 + j * 64 + l) * 16 : WS_CS + hg * 1536 + (j - 2) * 768 + (l < 48 ? l : 0) * 16;
    u32x4 g;
#pragma nounroll
    for (int sp = 0; sp < SPIN_CAP; sp++) {
      asm volatile("" ::: "memory");
      g = ld16<SC1>(rw, off, 0);
      if (__all(g[1] == tag && g[3] == tag)) break;
    }
    u32x2 o = {g[0], g[2]};
    if (j < 2 || l < 48) *(LAS u32x2*)(lds + (j < 2 ? G2_ST + j * 512 : G2_CS + (j - 2) * 384) + l * 8) = o;
  }
  wvb<0>();
}
DI void cs_phase(const Params& p, unsigned lds, int cg, int j, int pass) {
  const int l = threadIdx.x & 63;
  rsrc_t rq = mk(p.wqkv, D * 3 * D * 2), rw = mk(p.ws, WS_BYTES);
  float s[8] = {};
  unsigned sel_lo = 0x00003f80u;
  asm("" : "+s"(sel_lo));
  const unsigned vo = (j * 128 + (l >> 3)) * 3072 + (l & 7) * 16 + cg * 128;
  u32x4 v[16];
#pragma unroll
  for (int u = 0; u < 16; u++) v[u] = ld16(rq, vo, u * (8 * 3072));
  SB0();
#pragma unroll
  for (int u = 0; u < 16; u++)
#pragma unroll
    for (int jj = 0; jj < 4; jj++) { s[2 * jj] = dot2(v[u][jj], sel_lo, s[2 * jj]); s[2 * jj + 1] = dot2(v[u][jj], 0x3f800000u, s[2 * jj + 1]); }
  const unsigned sc = pass ? lds : lds + L_MISC;
  const unsigned pa = sc + (j * 8 + (l >> 3)) * 272 + (l & 7) * 32;
  f32x4 a = {s[0], s[1], s[2], s[3]}, b = {s[4], s[5], s[6], s[7]};
  *(LAS f32x4*)pa = a;
  *(LAS f32x4*)(pa + 16) = b;
  if (lds_arrive(lds + L_CTL + 28 + pass * 4, 4)) {
    float t = 0.f;
#pragma unroll
    for (int r = 0; r < 32; r++) t += *(LAS float*)(sc + r * 272 + l * 4);
    lds_wait(lds + L_CTL + 52, 1);
    u32x2 g = {asu(t), lds_tag(lds)};
    __builtin_amdgcn_raw_buffer_store_b64(g, rw, WS_CS + (cg & 7) * 1536 + (((l >> 5) * 3 + (cg >> 3)) * 32 + (l & 31)) * 8, 0, SC1);
  }
}

DI void g2_kv(const Params& p, unsigned lds, int rt, int hg, int w, int st, int dt2) {
  const int l = threadIdx.x & 63;
  rsrc_t rw = mk(p.ws, WS_BYTES);
  const int h = l >> 5, blk = (l >> 4) & 1, q = (l & 15) >> 2, pp = l & 3;
  const unsigned pwa = lds + L_PW + (l & 7) * 4;
    unsigned aa[2], ba[2], bs = blk ? 0 : 2048;
#pragma unroll
    for (int tt = 0; tt < 2; tt++) {
      unsigned sr = 8 * h + 4 * tt + q;
      unsigned f1 = ((sr & 3) << 3) | ((sr >> 1) & 7), f2 = ((sr << 1) | ((sr >> 3) & 1)) & 15;
      aa[tt] = lds + G2_PK + sr * 256 + (((w * 8 + blk * 4 + pp) ^ f1) * 8);
      ba[tt] = blk ? lds + G2_ONES + pp * 8 : lds + G2_V + sr * 128 + (((w * 4 + pp) ^ f2) * 8);
    }
    unsigned bb[8][2];
#pragma unroll
    for (int ks = 0; ks < 8; ks++) { bb[ks][0] = ba[0] + ks * bs; bb[ks][1] = ba[1] + ks * bs; asm volatile("" : "+v"(bb[ks][0]), "+v"(bb[ks][1])); }
    const int head = hg * 4 + w, c = l & 31;
    unsigned off = c < 16 ? WS_KVP + (rt * 32 + head) * 1024 + (h * 16 + c) * 16 : c == 16 ? WS_ZP + head * 1024 + (h * 16 + rt) * 16 : OOB;
    asm volatile("" : "+v"(off), "+v"(aa[0]), "+v"(aa[1]));
    bar();
    if (T_EARLY) {
      const TW tw(p, lds, st, dt2, w);
#pragma nounroll
      for (int s = 0; s < T_EARLY; s++) tw(s, s == 3 ? 4 : s == 4 ? 3 : s);
    } else
      __builtin_amdgcn_s_sleep(KV_DELAY);
    __builtin_amdgcn_s_setprio(2);
    prog_wait(pwa, 1);
    FSTW(6);
    bf16x8 ka[8], kb[8];
#pragma unroll
    for (int ks = 0; ks < 8; ks++) {
      ka[ks] = trd2(aa[0] + ks * 4096, aa[1] + ks * 4096);
      kb[ks] = trd2(bb[ks][0], bb[ks][1]);
    }
    SB0();
    f32x16 kv = {};
#pragma unroll
    for (int ks = 0; ks < 8; ks++) kv = mfma(ka[ks], kb[ks], kv);

    st16<SC1>(pack8u(kv, 0), rw, off);
    st16<SC1>(pack8u(kv, 1), rw, off + 512);
    drain();
    if (lds_arrive(lds + L_CTL, 4)) {
      if (l == 0) put_flag(rw, WS_F2 + (hg * 16 + rt) * 128, lds_tag(lds));
      FSTW(14);
    }
    __builtin_amdgcn_s_setprio(0);
}

DI void g2_compute(const Params& p, unsigned lds, int rt, int hg, int w) {
  const int t = threadIdx.x, l = t & 63;
  rsrc_t rw = mk(p.ws, WS_BYTES);
  if (t < 16) *(LAS unsigned*)(lds + G2_ONES + t * 4) = 0x3f803f80u;
  const int h = l >> 5, blk = (l >> 4) & 1, q = (l & 15) >> 2, pp = l & 3;
  const int mq = w >> 1, hp = w & 1;
  const unsigned xrow = mq * 32 + (l & 31);
  unsigned xa[4];
#pragma unroll
  for (int k = 0; k < 4; k++) xa[k] = (lds + G2_XO + xrow * 128 + ((h ^ ((xrow >> 1) & 7)) * 16)) ^ (k << 5);
  const unsigned wa_ = lds + G2_WO + (8 * h + q) * 128 + (((hp * 4 + blk * 2 + (pp >> 1)) ^ (((q >> 1) & 1) << 2)) * 16) + (pp & 1) * 8;
  f32x16 acc[3] = {};
  bf16x8 fx[4], fw[4][3];
  unsigned sb = 0;
  auto rd = [&](int k, int i) {
    if (i == 0) fx[k] = lds16(xa[k] + sb);
    const unsigned wb = wa_ + sb + k * 2048 + i * 8192;
    fw[k][i] = trd2(wb, wb + 512);
  };
  auto step = [&](int k, bool more) {
#pragma unroll
    for (int i = 0; i < 3; i++) {
      acc[i] = mfma(fw[k][i], fx[k], acc[i]);
      SB0();
      if (more) rd(k ^ 2, i);
      SB0();
    }
  };
  unsigned pka = (lds + G2_PK + xrow * 256 + (((xrow & 3) << 3) | ((xrow >> 1) & 7)) * 8) ^ ((hp * 16 + h) * 8);
  unsigned va = (lds + G2_V + xrow * 128 + (((xrow << 1) | ((xrow >> 3) & 1)) & 15) * 8) ^ ((hp * 8 + h) * 8);
  unsigned csa = lds + G2_CS + (hp * 96 + 4 * h) * 4;
  asm volatile("" : "+v"(acc[0]), "+v"(acc[1]), "+v"(acc[2]), "+v"(pka), "+v"(va), "+v"(csa));
  bar();
  STAMP(2);
#pragma unroll
  for (int i = 0; i < 6; i++) rd(i / 3, i % 3);
#pragma nounroll
  for (int s = 1; s < 8; s++) {
    step(0, true);
    step(1, true);
    sb = sb == 2 * G2_SLOT ? 0 : sb + G2_SLOT;
    bar();
    step(2, true);
    step(3, true);
  }
  step(0, true);
  step(1, true);
  bar();
  unsigned tch = 0;
  {
    const int u = (rt & 3) * 8 + hg, row = w * 8 + (l >> 3);
    rsrc_t rt_ = w < 6 ? mk(p.w2, F * S * 2) : mk(p.wo, D * D * 2);
    const unsigned o = w < 6 ? (row < 43 ? (u * 43 + row) * 4096 + (rt >> 2) * 1024 + (l & 7) * 128 : OOB) : (w == 6 && l < 32 ? ((rt * 8 + hg) * 32 + l) * 128 : OOB);
    asm volatile("buffer_load_dword %0, %1, %2, 0 offen" : "=v"(tch) : "v"(o), "s"(rt_) : "memory");
  }
  __builtin_amdgcn_s_setprio(1);
  step(2, false);
  f32x4 ck[4], cv[4];
#pragma unroll
  for (int i = 0; i < 4; i++) { ck[i] = *(LAS f32x4*)(csa + 128 + 32 * i); cv[i] = *(LAS f32x4*)(csa + 256 + 32 * i); }
  const f32x2 rm = *(LAS f32x2*)(lds + G2_ST + xrow * 8);
  bf16x8 wf[2];
#pragma unroll
  for (int e = 0; e < 2; e++) {
    unsigned a = lds + G2_PHI + (hp * 2 + e) * 512 + (4 * h + q) * 32 + (2 * blk + (pp >> 1)) * 8;
    wf[e] = trd2(a, a + 8 * 32);
  }
  SB0();
  acc[1] = mfma(fw[3][1], fx[3], acc[1]);
  acc[2] = mfma(fw[3][2], fx[3], acc[2]);
  acc[0] = mfma(fw[3][0], fx[3], acc[0]);
  SB0();
  STAMP(3);
#pragma unroll
  for (int e = 0; e < 2; e++) {
    u32x4 u = __builtin_bit_cast(u32x4, wf[e]);
    unsigned sgn = (q & 1) ? 0x80008000u : 0u;
#pragma unroll
    for (int jj = 0; jj < 4; jj++) u[jj] ^= sgn;
    wf[e] = __builtin_bit_cast(bf16x8, u);
  }
  const float r = rm[0], nm = -rm[1], sc = r * 1.4426950408889634f;
  const f32x16 z16 = {};
  {
    unsigned o[8];
    center<false>(acc[1], ck, nm, r, o);
    f32x16 pk0 = mfma(wf[0], frag(o, 0), z16), pk1 = mfma(wf[1], frag(o, 1), z16);
    SB0();
    center<true>(acc[2], cv, nm, r, o);
#pragma unroll
    for (int g = 0; g < 4; g++) *(LAS u32x2*)(va ^ (g * 16)) = u32x2{o[2 * g], o[2 * g + 1]};
    softmax16(pk0, sc, o);
#pragma unroll
    for (int g = 0; g < 4; g++) *(LAS u32x2*)(pka ^ (g * 16)) = u32x2{o[2 * g], o[2 * g + 1]};
    softmax16(pk1, sc, o);
#pragma unroll
    for (int g = 0; g < 4; g++) *(LAS u32x2*)(pka ^ (64 + g * 16)) = u32x2{o[2 * g], o[2 * g + 1]};
  }
  const unsigned pwm = lds + L_PW + w * 4, pwa = lds + L_PW + (l & 7) * 4;
  prog_put(pwm, 1);
  SB0();
  STAMP(5);
  __builtin_amdgcn_s_setprio(0);
  bf16x8 phq[2][2];
  {
    f32x4 cq[4];
#pragma unroll
    for (int i = 0; i < 4; i++) cq[i] = *(LAS f32x4*)(csa + 32 * i);
    unsigned o[8];
    center<false>(acc[0], cq, nm, r, o);
    f32x16 pq0 = mfma(wf[0], frag(o, 0), z16), pq1 = mfma(wf[1], frag(o, 1), z16);
    softmax16(pq0, sc, o);
    phq[0][0] = frag(o, 0); phq[0][1] = frag(o, 1);
    softmax16(pq1, sc, o);
    phq[1][0] = frag(o, 0); phq[1][1] = frag(o, 1);
  }
  const int hh = w & 3, ee = w >> 2, head = hg * 4 + hh;
  unsigned ko = WS_KVP + ((l >> 5) * 32 + head) * 1024 + ee * 512 + (l & 31) * 16, zo = l < 32 ? OOB : WS_ZP + head * 1024 + ee * 512 + (l & 31) * 16;
  unsigned ta = lds + G2_T + hh * 2048 + ee * 1024 + (((l >> 4) & 1) * 32 + (l >> 5) * 16 + (l & 15)) * 16;
  unsigned tr_ = lds + G2_T + hp * 4096 + l * 16, oo = WS_O + l * 16 + ((rt * 4 + mq) * 32 + hg * 4 + hp * 2) * 1024;
  asm volatile("" : "+v"(ko), "+v"(zo), "+v"(ta), "+v"(tr_), "+v"(oo), "+v"(phq[0][0]), "+v"(phq[0][1]), "+v"(phq[1][0]), "+v"(phq[1][1]));
  if (w == 7) {
    lds_wait(lds + L_CTL, 4);
    __builtin_amdgcn_s_sleep(LN_DELAY);
    poll_flags(rw, WS_F2 + (hg * 16 + (l & 15)) * 128, lds_tag(lds));
    lds_set(lds + L_CTL + 12);
  }
  lds_wait(lds + L_CTL + 12, 1);
  STAMP(8);
  {
    u32x4 v[8];
#pragma unroll
    for (int i = 0; i < 8; i++) v[i] = ld16<SC1>(rw, ko, i * 65536);
    u32x4 zv = ld16<SC1>(rw, zo, 0);
    FSTV(10, zv[0]);
    float s[8] = {};
    unsigned sel_lo = 0x00003f80u;
    asm("" : "+s"(sel_lo));
#pragma unroll
    for (int i = 0; i < 8; i++)
#pragma unroll
      for (int jj = 0; jj < 4; jj++) { s[2 * jj] = dot2(v[i][jj], sel_lo, s[2 * jj]); s[2 * jj + 1] = dot2(v[i][jj], 0x3f800000u, s[2 * jj + 1]); }
#pragma unroll
    for (int i = 0; i < 8; i++) s[i] = half_sum(s[i]);
    u32x4 o = {pk(s[0], s[1]), pk(s[2], s[3]), pk(s[4], s[5]), pk(s[6], s[7])};
    asm volatile("" : "+v"(o) : "v"(tch));
    if (l >= 32) o = zv;
    *(LAS u32x4*)ta = o;
  }
  FST(11);
  prog_put(pwm, 2);
  prog_wait(pwa, 2);
  FST(12);
  bf16x8 ta_[2][2];
#pragma unroll
  for (int e = 0; e < 2; e++)
#pragma unroll
    for (int c = 0; c < 2; c++) ta_[e][c] = lds16(tr_ + e * 2048 + c * 1024);
  SB0();
  f32x16 a[2];
#pragma unroll
  for (int e = 0; e < 2; e++) a[e] = mfma(ta_[e][1], phq[e][1], mfma(ta_[e][0], phq[e][0], z16));
  FSTV(13, a[1][0]);
#pragma unroll
  for (int e = 0; e < 2; e++) {
    const f32x2 d2 = (f32x2{a[e][8], a[e][9]} + f32x2{a[e][10], a[e][11]}) + (f32x2{a[e][12], a[e][13]} + f32x2{a[e][14], a[e][15]});
    const float den = __builtin_amdgcn_rcpf(half_sum(d2[0] + d2[1]) + 1e-6f);
    u32x4 o;
#pragma unroll
    for (int i = 0; i < 4; i++) { const f32x2 u = f32x2{a[e][2 * i], a[e][2 * i + 1]} * den; o[i] = pk(u[0], u[1]); }
    st16<SC1>(o, rw, oo + e * 1024);
  }
  STAMP(15);
}

DI void p2_touch(const Params& p, int st, int dt) {
  const int l = threadIdx.x & 63;
  rsrc_t rw2 = mk(p.w2, F * S * 2), rwo = mk(p.wo, D * D * 2);
  unsigned sink = 0;
#pragma nounroll
  for (int k = 0; k < 11; k++) sink += __builtin_amdgcn_raw_buffer_load_b32(rw2, (l >> 3) * 4096 + (l & 7) * 128, (k * 16 + (dt & 1) * 8 + (st & 7)) * (8 * 4096) + (st >> 3) * 1024, 0);
#pragma nounroll
  for (int k = 0; k < 2; k++) sink += __builtin_amdgcn_raw_buffer_load_b32(rwo, (l >> 2) * 1024 + (l & 3) * 128, (k * 16 + (dt & 1) * 8 + (st & 7)) * (16 * 1024) + (dt >> 2) * 512, 0);
  asm volatile("" ::"v"(sink));
}
DI void p2_loader(const Params& p, unsigned lds, int st, int dt, int j, bool g1t) {
  const int l = threadIdx.x & 63;
  rsrc_t rwo = mk(p.wo, D * D * 2), rw = mk(p.ws, WS_BYTES);
  const unsigned tvo = (j * 32 + (l >> 3)) * 1024 + ((l & 7) ^ (((l >> 4) & 1) << 2)) * 16, base = lds + j * 4096;
  const TW tw(p, lds, st, dt, j);
  const int rot = tw.rot;
  bar();
  tw(6, 6); tw(7, 7); tw(8, 0); tw(9, 1); tw(10, 2);
  wvb<0>();
  STAMPW(6, j == 0);
  bar();
  STAMPW(7, j == 0);
  auto two = [&](int u) {
    const unsigned to = (((u >> 1) + rot) & 3) * (128 * 1024) + dt * 128 + (u & 1) * (16 * 1024), dst = base + (u >> 1) * P2_S + (u & 1) * 2048;
    dma16(rwo, tvo, to, dst);
    dma16(rwo, tvo, to + 8 * 1024, dst + 1024);
  };
#pragma nounroll
  for (int u = 0; u < 8; u++) two(u);
  const unsigned g0 = WS_G + (dt * 2 + (j >> 1)) * GT * 1024 + (j & 1) * 4096, o0 = WS_O + (st * 2 + (j >> 1)) * 32768 + (j & 1) * 4096;
  unsigned gc = (rot % 11) * 8192, vl = l * 16;
  const unsigned f3 = WS_F3 + (dt * 16 + ((l & 15) < 15 ? (l & 15) : 0)) * 128, tag = lds_tag(lds);
  auto issue = [&](int s) {
    if (s >= 4) { dma16x4_sc1(rw, vl, g0 + gc, base + (4 + s % 5) * P2_S); gc = gc == 10 * 8192 ? 0 : gc + 8192; }
    else dma16x4_sc1(rw, vl, o0 + ((s + rot) & 3) * 8192, base + (4 + s % 5) * P2_S);
  };
  bar();
  STAMPW(10, j == 0);
  u32x2 fg;
  asm volatile("buffer_load_dwordx2 %0, %1, %2, 0 offen sc1" : "=v"(fg) : "v"(f3), "s"(rw) : "memory");
#pragma unroll
  for (int s = 0; s < 4; s++) issue(s);
  wvb<12>();
  asm volatile("" : "+v"(fg));
  if (__all(fg[0] == tag && fg[1] == ~tag)) {
#pragma unroll
    for (int s = 0; s < 4; s++) { issue(s + 4); wvb<12>(); }
  } else {
    wvb<8>(); wvb<4>(); wvb<0>();
    poll_flags(rw, f3, tag);
    STAMPW(14, j == 0);
#pragma unroll
    for (int s = 4; s < 8; s++) issue(s);
    wvb<12>();
  }
#pragma unroll
  for (int s = 4; s < 11; s++) { issue(s + 4); wvb<12>(); }
  wvb<8>(); wvb<4>(); wvb<0>();
  bar();
  bar();
}
DI void p2_compute(const Params& p, unsigned lds, int st, int dt, int w, unsigned pflag, bool g1t, TP& tp) {
  const int t = threadIdx.x, l = t & 63;
  rsrc_t rx = mk(p.x, S * D * 2), rb2 = mk(p.b2, S * 2), ro = mk(p.out, S * D * 2);
  const unsigned ov = (t >> 3) * 1024 + (t & 7) * 16 + st * 65536 + dt * 128;
  const int h = l >> 5, blk = (l >> 4) & 1, q = (l & 15) >> 2, pp = l & 3;
  const int kq = w >> 1, ms = w & 1;
  const unsigned t0 = p2_t0(lds, w), tm = t0 ^ (ms * 64), t1 = t0 ^ 64;
  rsrc_t rw = mk(p.ws, WS_BYTES);
  drain();
  if (pflag) {
    if (lds_arrive(lds + L_CTL + 56, g1t ? 6 : 8)) {
      if (l == 0) put_flag(rw, pflag, lds_tag(lds));
    }
    STAMP(9);
  }
  u32x4 xres;
  unsigned short b2v;
  bar();
  pull(tp, 4, tm + 3 * P2_S); pull(tp, 8, tm); pull(tp, 9, tm + P2_S); pull(tp, 10, tm + 2 * P2_S);
  bar();
  pull(tp, 5, tm + (g1t ? 8 : 5) * P2_S); pull(tp, 6, tm + 6 * P2_S); pull(tp, 7, tm + 7 * P2_S);
  f32x16 acc[2] = {};
  asm volatile("" : "+v"(acc[0]), "+v"(acc[1]));
  if (w == 7) poll_flags(rw, WS_F4 + ((st >> 1) * 8 + (l & 7)) * 128, lds_tag(lds));
  bar();
  const unsigned la = lds + 4 * P2_S + kq * 2048 + l * 16, la4 = la + 4 * P2_S;
  bf16x8 fa[2][2][2], fo[2][2];
  auto rd = [&](int s, int k) {
    const int buf = s & 1, sl = s % 5;
    const unsigned a = (sl < 4 ? la + sl * P2_S : la4) + k * 1024;
    if (s >= 4) {
      fa[buf][k][0] = lds16(a);
      fa[buf][k][1] = lds16(a + 8192);
    } else {
      fo[buf][k] = lds16(a + ms * 8192);
      const unsigned o = s * P2_S + k * 2048;
      fa[buf][k][0] = trd2(t0 + o, t0 + o + 1024);
      fa[buf][k][1] = trd2(t1 + o, t1 + o + 1024);
    }
  };
  bar();
  STAMP(11);
  rd(0, 0); rd(0, 1);
#pragma unroll
  for (int s = 0; s < 15; s++) {
    bar();
#pragma unroll
    for (int k = 0; k < 2; k++) {
      const bf16x8 bop = s >= 4 ? tp.f[s >= 4 ? s - 4 : 0][k] : fo[s & 1][k];
      acc[0] = mfma(fa[s & 1][k][0], bop, acc[0]);
      SB0();
      if (s >= 4 && s < 14 && k == 0) { rd(s + 1, 0); rd(s + 1, 1); }
      if (s < 4 && k == 1) rd(s + 1, 1);
      SB0();
      acc[1] = mfma(fa[s & 1][k][1], bop, acc[1]);
      SB0();
      if (s < 4 && k == 0) rd(s + 1, 0);
      if (s == 7 && k == 0) {
        xres = ld16(rx, ov, 0);
        b2v = __builtin_amdgcn_raw_buffer_load_b16(rb2, (t >> 3) * 2, st * 128, 0);
      }
      SB0();
    }
  }
  STAMP(12);
#pragma unroll
  for (int nd = 0; nd < 2; nd++)
#pragma unroll
    for (int g = 0; g < 4; g++) {
      f32x4 v = {acc[nd][4 * g], acc[nd][4 * g + 1], acc[nd][4 * g + 2], acc[nd][4 * g + 3]};
      *(LAS f32x4*)(lds + kq * 17408 + (ms * 32 + (l & 31)) * 272 + (nd * 32 + 8 * g + 4 * h) * 4) = v;
    }
  bar();
  {
    const unsigned ra = lds + (t >> 3) * 272 + (t & 7) * 32;
    f32x4 a0 = (*(LAS f32x4*)ra + *(LAS f32x4*)(ra + 17408)) + (*(LAS f32x4*)(ra + 2 * 17408) + *(LAS f32x4*)(ra + 3 * 17408));
    f32x4 a1 = (*(LAS f32x4*)(ra + 16) + *(LAS f32x4*)(ra + 17408 + 16)) + (*(LAS f32x4*)(ra + 2 * 17408 + 16) + *(LAS f32x4*)(ra + 3 * 17408 + 16));
    const float bb = blo(b2v);
    u32x4 o = {pk(a0[0] + bb + blo(xres[0]), a0[1] + bb + bhi(xres[0])), pk(a0[2] + bb + blo(xres[1]), a0[3] + bb + bhi(xres[1])),
               pk(a1[0] + bb + blo(xres[2]), a1[1] + bb + bhi(xres[2])), pk(a1[2] + bb + blo(xres[3]), a1[3] + bb + bhi(xres[3]))};
    st16<OUT_AUX>(o, ro, ov);
  }
  STAMP(13);
}

extern __shared__ __attribute__((aligned(128))) char smem[];
DI unsigned lds_base() { return (unsigned)(size_t)(LAS char*)smem; }

__global__ __launch_bounds__(NT, 3) void k_main(Params p) {
  const int b = blockIdx.x, xr = (b + XROT) & 7, i = b >> 3, l = threadIdx.x & 63, w = __builtin_amdgcn_readfirstlane(threadIdx.x >> 6);
  const unsigned lds = lds_base();
  STAMP(0);
  rsrc_t rw = mk(p.ws, WS_BYTES);
  const int st = (xr >> 1) * 8 + (i & 7), dt2 = (xr & 1) * 4 + (i >> 3);
  const bool g1 = i < 16;
  const int gx = xr & 3;
  const bool crew = g1 && (w < 6 || w >= 10);
  const int c = w < 6 ? w : w - 4, lb = xr * 16 + (i & 15);
  u32x4 v0, v1;
  if (crew) {
    if (CPRIO) __builtin_amdgcn_s_setprio(CPRIO);
    rsrc_t rx = mk(p.x, S * D * 2);
    const unsigned o = (lb * 16 + c * 2 + (l >> 5)) * 1024 + (l & 31) * 16;
    v0 = ld16(rx, o, 0);
    v1 = ld16(rx, o + 512, 0);
  }
  unsigned seedv = 0;
  if (crew || w == 0) seedv = __builtin_amdgcn_raw_buffer_load_b32(rw, WS_SEED, 0, SC1);
  if (threadIdx.x < 128) *(LAS unsigned*)(lds + L_CTL + threadIdx.x * 4) = 0u;
  bar();
  if (w == 0) { *(volatile LAS unsigned*)(lds + L_CTL + 24) = seedv + 1u; lds_set(lds + L_CTL + 52); }
  unsigned pflag = 0;
  bool g1t = false, pulled = false;
  TP tp;
  if (g1) {
    const int dt = (xr & 1) * 4 + (i & 3), ft = (xr >> 1) * 4 + (i >> 2);
    const int rot = ((2 * ((i & 3) + (i >> 2)) + (xr >> 1)) & 7) | ((xr & 1) << 3);
    const bool bumper = xr == 6 && i == 12;
    if (!crew) {
      if (ft >= 14) cs_phase(p, lds, (ft - 14) * 8 + dt, w - 6, 0);
      if (ft == 15) cs_phase(p, lds, 16 + dt, w - 6, 1);
      if (ft < 15) g1_wloader(p, lds, ft, rot, w - 6, st, dt2);
    } else {
      const unsigned tag = __builtin_amdgcn_readfirstlane(seedv) + 1u;
      ln_phase(p, lds, lb, c, tag, v0, v1);
      STAMP(1);
      if (ft < 15) {
        if (w < 6) { __builtin_amdgcn_s_setprio(0); if (G1PRIO == 1 && w >= 4) __builtin_amdgcn_s_setprio(1); if (G1PRIO == 2) __builtin_amdgcn_s_setprio(1); g1_compute(p, lds, dt, ft, w, tp); pulled = true; pflag = WS_F3 + (dt * 16 + ft) * 128; STAMP(5); }
        else { g1_xloader(p, lds, dt, rot, w - 10, tag); bar(); }
      } else if (bumper && w == 0) {
        poll_ln(rw, tag);
        poll_flags(rw, WS_F2 + l * 128, tag);
        poll_flags(rw, WS_F2 + 8192 + l * 128, tag);
        if (l == 0) __builtin_amdgcn_raw_buffer_store_b32(tag, rw, WS_SEED, 0, SC1);
      }
    }
    g1t = ft < 15;
    if (!g1t) {
      bar();
      if (w >= 8) {
        const TW tw(p, lds, st, dt2, w - 8);
#pragma nounroll
        for (int s = 0; s < 6; s++) tw(s, s == 3 ? 4 : s == 4 ? 3 : s);
      }
      wvb<0>();
    }
  } else {
    const int rt = (xr >> 1) * 4 + (i & 3), hg = (xr & 1) * 4 + ((i >> 2) & 3), rot = G2ROT == 0 ? xr : G2ROT == 1 ? (xr + ((i + (i >> 2)) & 1)) & 7 : ((i & 3) * 2 + ((i >> 2) & 3) * 3) & 7;
    if (w >= 8) {
      g2_loader(p, lds, rt, hg, rot, w - 8);
      g2_kv(p, lds, rt, hg, w - 8, st, dt2);
      if (w == 11 && T_GATE) {
        lds_wait(lds + L_CTL, 4);
        __builtin_amdgcn_s_sleep(LN_DELAY);
        poll_flags(rw, WS_F2 + (hg * 16 + (l & 15)) * 128, lds_tag(lds));
        lds_set(lds + L_CTL + 12);
      }
      if (T_GATE) { lds_wait(lds + L_CTL + 12, 1); prog_wait(lds + L_PW + (l & 7) * 4, 2); }
      const TW tw(p, lds, st, dt2, w - 8);
#pragma nounroll
      for (int s = T_EARLY; s < 6; s++) { tw(s, s == 3 ? 4 : s == 4 ? 3 : s); wv<T_PACE>(); if (T_SLEEP) __builtin_amdgcn_s_sleep(T_SLEEP); }
      STAMPW(1, w == 8);
      if (w == 11 && !T_GATE && !KV_CPOLL) {
        lds_wait(lds + L_CTL, 4);
        __builtin_amdgcn_s_sleep(LN_DELAY);
        poll_flags(rw, WS_F2 + (hg * 16 + (l & 15)) * 128, lds_tag(lds));
        lds_set(lds + L_CTL + 12);
      }
    } else {
      g2_compute(p, lds, rt, hg, w);
      pflag = WS_F4 + (rt * 8 + hg) * 128;
    }
    if (w >= 8) wvb<0>(); else bar();
  }
  if (w < 8) {
    if (!pulled) {
      pull03(tp, p2_t0(lds, w) ^ ((w & 1) * 64), g1t);
      bar();
    }
    p2_compute(p, lds, st, dt2, w, pflag, g1t, tp);
  }
  else p2_loader(p, lds, st, dt2, w - 8, g1t);
}
}

extern "C" void kernel_launch(void* const* d_in, const int* in_sizes, int n_in, void* d_out, int out_size, void* d_ws, size_t ws_size,
                              hipStream_t stream) {
  using namespace hk;
  Params p{d_in[0], d_in[1], d_in[2], d_in[3], d_in[5], d_in[6], d_in[7], d_in[8], d_out, d_ws};
  (void)hipFuncSetAttribute((const void*)k_main, hipFuncAttributeMaxDynamicSharedMemorySize, LDS_BYTES);
  k_main<<<256, NT, LDS_BYTES, stream>>>(p);
}
```

```cpp
#include <hip/hip_runtime.h>
#include <hip/hip_bf16.h>

namespace hk {
#define DI __device__ __forceinline__
#define LAS __attribute__((address_space(3)))
typedef short bf16x8 __attribute__((ext_vector_type(8)));
typedef short s16x4 __attribute__((ext_vector_type(4)));
typedef float f32x16 __attribute__((ext_vector_type(16)));
typedef float f32x4 __attribute__((ext_vector_type(4)));
typedef float f32x2 __attribute__((ext_vector_type(2)));
typedef unsigned u32x4 __attribute__((ext_vector_type(4)));
typedef unsigned u32x2 __attribute__((ext_vector_type(2)));
typedef __bf16 bfx2 __attribute__((ext_vector_type(2)));
typedef __amdgpu_buffer_rsrc_t rsrc_t;

constexpr int S = 2048, D = 512, F = 1376;
constexpr int GT = 88;
constexpr int WS_SEED = 0;
constexpr int WS_F2 = 4096;
constexpr int WS_F3 = 20480;
constexpr int WS_F4 = 36864;
constexpr int WS_CS = 53248;
constexpr int WS_SB = 65536;
constexpr int WS_ZP = 98304;
constexpr int WS_KVP = 131072;
constexpr int WS_G = 1 << 20;
constexpr int WS_O = 3 << 20;
constexpr int WS_H = 5 << 20;
constexpr int WS_BYTES = 7 << 20;
constexpr int SC1 = 16;
constexpr int NT = 768;
constexpr unsigned OOB = 0x40000000u;
constexpr int SPIN_CAP = 1 << 14;
constexpr int POLL_DELAY = 12;
constexpr int IDLE_SLP = 40;
constexpr int CODE_BYTES = 31744;

struct Params {
  const void *x, *wqkv, *wphi, *wo, *w1, *b1, *w2, *b2;
  void *out, *ws;
};

#define SB0() __builtin_amdgcn_sched_barrier(0)
DI rsrc_t mk(const void* p, int bytes) { return __builtin_amdgcn_make_buffer_rsrc((void*)p, 0, bytes, 0x00020000); }
DI void dma16(rsrc_t r, unsigned voff, unsigned soff, unsigned lds) {
  asm volatile("s_mov_b32 m0, %0\n\ts_nop 0\n\tbuffer_load_dwordx4 %1, %2, %3 offen lds" ::"s"(lds), "v"(voff), "s"(r), "s"(soff) : "memory");
}
DI void dma16x4_sc1(rsrc_t r, unsigned voff, unsigned soff, unsigned lds) {
  asm volatile("s_mov_b32 m0, %0\n\ts_nop 0\n\tbuffer_load_dwordx4 %1, %2, %3 offen sc1 lds\n\tbuffer_load_dwordx4 %1, %2, %3 offen offset:1024 sc1 lds\n\t"
               "buffer_load_dwordx4 %1, %2, %3 offen offset:2048 sc1 lds\n\tbuffer_load_dwordx4 %1, %2, %3 offen offset:3072 sc1 lds" ::"s"(lds), "v"(voff), "s"(r), "s"(soff) : "memory");
}
DI void lds_set(unsigned a) { *(volatile LAS unsigned*)a = 1u; }
template <int N> DI void wvb() { asm volatile("s_waitcnt vmcnt(%0) lgkmcnt(0)\n\ts_barrier" ::"i"(N) : "memory"); }
DI void bar() { asm volatile("s_waitcnt lgkmcnt(0)\n\ts_barrier" ::: "memory"); }
DI void drain() { asm volatile("s_waitcnt vmcnt(0)" ::: "memory"); }
template <int AUX = 0> DI u32x4 ld16(rsrc_t r, unsigned voff, unsigned soff) { return __builtin_amdgcn_raw_buffer_load_b128(r, voff, soff, AUX); }
template <int AUX = 0> DI u32x2 ld8(rsrc_t r, unsigned voff, unsigned soff) { return __builtin_amdgcn_raw_buffer_load_b64(r, voff, soff, AUX); }
template <int AUX = 0> DI void st16(u32x4 v, rsrc_t r, unsigned voff) { __builtin_amdgcn_raw_buffer_store_b128(v, r, voff, 0, AUX); }
DI s16x4 trd(unsigned a) { return __builtin_amdgcn_ds_read_tr16_b64_v4i16((LAS s16x4*)a); }
DI bf16x8 trd2(unsigned a0, unsigned a1) {
  s16x4 lo = trd(a0), hi = trd(a1);
  return __builtin_shufflevector(lo, hi, 0, 1, 2, 3, 4, 5, 6, 7);
}
DI bf16x8 lds16(unsigned a) { return *(LAS bf16x8*)a; }
DI f32x16 mfma(bf16x8 a, bf16x8 b, f32x16 c) { return __builtin_amdgcn_mfma_f32_32x32x16_bf16(a, b, c, 0, 0, 0); }
DI unsigned pk(float lo, float hi) {
  f32x2 v = {lo, hi};
  return __builtin_bit_cast(unsigned, __builtin_convertvector(v, bfx2));
}
DI float asf(unsigned u) { return __builtin_bit_cast(float, u); }
DI unsigned asu(float u) { return __builtin_bit_cast(unsigned, u); }
DI float blo(unsigned u) { return asf(u << 16); }
DI float bhi(unsigned u) { return asf(u & 0xffff0000u); }
DI u32x4 pack8u(const f32x16& x, int s) {
  u32x4 p = {pk(x[8 * s], x[8 * s + 1]), pk(x[8 * s + 2], x[8 * s + 3]), pk(x[8 * s + 4], x[8 * s + 5]), pk(x[8 * s + 6], x[8 * s + 7])};
  return p;
}
DI float dot2(unsigned a, unsigned b, float c) { return __builtin_amdgcn_fdot2_f32_bf16(__builtin_bit_cast(bfx2, a), __builtin_bit_cast(bfx2, b), c, false); }
DI float half_sum(float v) {
  unsigned u = asu(v);
  auto r = __builtin_amdgcn_permlane32_swap(u, u, false, false);
  return asf(r[0]) + asf(r[1]);
}
template <int C> DI float dpp_add(float v) { return v + __builtin_amdgcn_update_dpp(0.f, v, C, 0xf, 0xf, false); }
DI float half32_sum(float v) {
  v = dpp_add<0x128>(v);
  v = dpp_add<0x124>(v);
  v = dpp_add<0x122>(v);
  v = dpp_add<0x121>(v);
  unsigned u = asu(v);
  auto r = __builtin_amdgcn_permlane16_swap(u, u, false, false);
  return asf(r[0]) + asf(r[1]);
}
DI unsigned next_tag(unsigned seed) { return seed * 1664525u + 1013904223u; }
DI void put_flag(rsrc_t rw, unsigned off, unsigned tag) {
  u32x2 f = {tag, ~tag};
  __builtin_amdgcn_raw_buffer_store_b64(f, rw, off, 0, SC1);
}
template <int SLP = 1> DI void poll_flags(rsrc_t rw, unsigned off, unsigned tag) {
#pragma nounroll
  for (int sp = 0; sp < SPIN_CAP; sp++) {
    asm volatile("" ::: "memory");
    u32x2 f = ld8<SC1>(rw, off, 0);
    if (__all(f[0] == tag && f[1] == ~tag)) break;
    __builtin_amdgcn_s_sleep(SLP);
  }
  asm volatile("" ::: "memory");
}
DI bool lds_arrive(unsigned cnt, unsigned n) {
  asm volatile("s_waitcnt lgkmcnt(0)" ::: "memory");
  unsigned old = 0;
  if ((threadIdx.x & 63) == 0) old = __hip_atomic_fetch_add((LAS unsigned*)cnt, 1u, __ATOMIC_RELAXED, __HIP_MEMORY_SCOPE_WORKGROUP);
  old = __builtin_amdgcn_readfirstlane(old);
  asm volatile("" ::: "memory");
  return old == n - 1;
}
DI void lds_wait(unsigned cnt, unsigned n) {
  while (__hip_atomic_load((LAS unsigned*)cnt, __ATOMIC_RELAXED, __HIP_MEMORY_SCOPE_WORKGROUP) < n) __builtin_amdgcn_s_sleep(1);
  asm volatile("" ::: "memory");
}

DI void prog_put(unsigned a, unsigned v) { asm volatile("" ::: "memory"); *(volatile LAS unsigned*)a = v; asm volatile("" ::: "memory"); }
DI void prog_wait(unsigned a, unsigned n) {
  unsigned seen;
  asm volatile(
      ".Lpw_loop%=:\n\tds_read_b32 %0, %1\n\ts_waitcnt lgkmcnt(0)\n\t"
      "v_cmp_le_u32 vcc, %2, %0\n\ts_cmp_eq_u64 vcc, exec\n\ts_cbranch_scc0 .Lpw_loop%="
      : "=&v"(seen) : "v"(a), "s"(n) : "vcc", "scc", "memory");
}
constexpr int L_CTL = 147456;
constexpr int L_PW = L_CTL + 320;
constexpr int L_MISC = L_CTL + 512;
constexpr int LDS_BYTES = L_MISC + 9216;
constexpr int G1_W = 0, G1_WS = 24576, G1_X = 4 * G1_WS, G1_XS = 16384;
constexpr int G2_SLOT = 40960, G2_XO = 0, G2_WO = 16384, G2_PHI = L_MISC, G2_ONES = G2_PHI + 2048, G2_CS = G2_ONES + 64, G2_ST = G2_CS + 768;
constexpr int G2_PK = 98304, G2_V = G2_PK + 32768, G2_T = G2_V;
constexpr int P2_S = 16384;
static_assert(G2_ST + 1024 <= LDS_BYTES && LDS_BYTES <= 163840, "lds");

DI unsigned lds_tag(unsigned lds) { return __builtin_amdgcn_readfirstlane(*(volatile LAS unsigned*)(lds + L_CTL + 24)); }

DI void ln_phase(const Params& p, unsigned lds, int lb, int c, unsigned tag, u32x4 v0, u32x4 v1) {
  const int l = threadIdx.x & 63;
  rsrc_t rw = mk(p.ws, WS_BYTES);
  float s = 0.f, s1 = 0.f;
#pragma unroll
  for (int i = 0; i < 4; i++) { s = dot2(v0[i], 0x3f803f80u, s); s1 = dot2(v1[i], 0x3f803f80u, s1); }
  const float m = half32_sum(s + s1) * (1.0f / D);
  f32x2 e[8], se = {0.f, 0.f}, sq = {0.f, 0.f};
#pragma unroll
  for (int i = 0; i < 4; i++) {
    e[i] = f32x2{blo(v0[i]), bhi(v0[i])} - m;
    e[4 + i] = f32x2{blo(v1[i]), bhi(v1[i])} - m;
  }
#pragma unroll
  for (int i = 0; i < 8; i++) { se += e[i]; sq += e[i] * e[i]; }
  const float me = half32_sum(se[0] + se[1]) * (1.0f / D), var = half32_sum(sq[0] + sq[1]) * (1.0f / D);
  const float r = __builtin_amdgcn_rsqf(var + 1e-5f);
  const float m2 = me * r, v2 = fmaxf(var - me * me, 0.f) * r * r;
  const float r2 = __builtin_amdgcn_rsqf(v2 + 1e-5f);
  const float ca = r * r2, cb = -m2 * r2;
  u32x4 o0, o1;
#pragma unroll
  for (int i = 0; i < 4; i++) {
    f32x2 u = e[i] * ca + cb, u2 = e[4 + i] * ca + cb;
    o0[i] = pk(u[0], u[1]);
    o1[i] = pk(u2[0], u2[1]);
  }
  const int row = c * 2 + (l >> 5);
  const unsigned ho = WS_H + ((l & 31) >> 3) * (S * 128) + (lb * 16 + row) * 128 + (((l & 7) ^ ((c & 1) << 2)) * 16);
  st16<SC1>(o0, rw, ho);
  st16<SC1>(o1, rw, ho + 4 * (S * 128));
  if ((l & 31) == 0) {
    u32x4 g = {asu(r), tag, asu(m), ~tag};
    *(LAS u32x4*)(lds + L_CTL + 64 + row * 16) = g;
  }
  drain();
  if (lds_arrive(lds + L_CTL + 4, 8)) {
    if (l < 16) {
      u32x4 g = *(LAS u32x4*)(lds + L_CTL + 64 + l * 16);
      st16<SC1>(g, rw, WS_SB + lb * 256 + l * 16);
    }
    lds_set(lds + L_CTL + 12);
  }
}
template <int SLP = 1> DI void poll_ln(rsrc_t rw, unsigned tag) {
  const int l = threadIdx.x & 63;
  bool ok0 = false, ok1 = false;
#pragma nounroll
  for (int sp = 0; sp < SPIN_CAP; sp++) {
    asm volatile("" ::: "memory");
    u32x4 f0 = ld16<SC1>(rw, ok0 ? OOB : WS_SB + l * 256, 0), f1 = ld16<SC1>(rw, ok1 ? OOB : WS_SB + 16384 + l * 256, 0);
    ok0 = ok0 || (f0[1] == tag && f0[3] == ~tag);
    ok1 = ok1 || (f1[1] == tag && f1[3] == ~tag);
    if (__all(ok0 && ok1)) break;
    __builtin_amdgcn_s_sleep(SLP);
  }
  asm volatile("" ::: "memory");
}

DI int g1_stage(int s, int rot) { return ((s + (rot & 8)) & 8) | ((s + rot) & 7); }
DI int p2_rot(int st, int dt) { return (((st & 7) * 11) >> 3) + 6 * (dt & 1) + 3 * ((dt >> 1) & 1); }
struct TW {
  rsrc_t rw2;
  unsigned tv2, base;
  int rot, st;
  DI TW(const Params& p, unsigned lds, int st_, int dt, int j) {
    const int l = threadIdx.x & 63;
    rw2 = mk(p.w2, F * S * 2);
    tv2 = (j * 32 + (l >> 3)) * 4096 + ((l & 7) ^ (((l >> 4) & 1) << 2)) * 16;
    base = lds + j * 4096; rot = p2_rot(st_, dt); st = st_;
  }
  DI void operator()(int s, int slot) const {
    const unsigned to = ((s + rot) % 11) * (128 * 4096) + st * 128;
#pragma unroll
    for (int i = 0; i < 4; i++) dma16(rw2, tv2, to + i * (8 * 4096), base + slot * P2_S + i * 1024);
  }
};
struct TP {
  bf16x8 f[11][2];
};
DI unsigned p2_t0(unsigned lds, int w) {
  const int l = threadIdx.x & 63, h = l >> 5, blk = (l >> 4) & 1, q = (l & 15) >> 2, pp = l & 3;
  return lds + ((w >> 1) * 32 + 4 * h + q) * 128 + (((blk * 2 + (pp >> 1)) ^ (((q >> 1) & 1) << 2)) * 16) + (pp & 1) * 8;
}
DI void pull(TP& t, int s, unsigned a) {
  t.f[s][0] = trd2(a, a + 1024);
  t.f[s][1] = trd2(a + 2048, a + 3072);
}
DI void pull03(TP& t, unsigned tm, bool g1t) {
  pull(t, 0, tm); pull(t, 1, tm + P2_S); pull(t, 2, tm + 2 * P2_S); pull(t, 3, tm + (g1t ? 7 : 4) * P2_S);
}
DI void g1_wloader(const Params& p, unsigned lds, int ft, int rot, int j, int st, int dt2) {
  const int l = threadIdx.x & 63;
  rsrc_t rw1 = mk(p.w1, S * F * 2);
  unsigned wvo[3];
#pragma unroll
  for (int i = 0; i < 3; i++) {
    unsigned u = i * 64 + l;
    unsigned row = u / 12, ch = u - row * 12;
    wvo[i] = (ft * 96 + ch * 8 >= F) ? OOB : (j * 32 + row) * (F * 2) + ch * 16;
  }
  const unsigned wl = lds + G1_W + j * 6144;
  auto issue = [&](int q) {
    const unsigned so = g1_stage(q, rot) * (128 * F * 2) + ft * 192, dst = wl + (q & 3) * G1_WS;
#pragma unroll
    for (int k = 0; k < 6; k++) dma16(rw1, wvo[k % 3], so + (k / 3) * (16 * F * 2), dst + k * 1024);
  };
  lds_wait(lds + L_CTL + 12, 1);
#pragma nounroll
  for (int q = 0; q < 4; q++) issue(q);
  wvb<18>();
  wvb<12>();
#pragma nounroll
  for (int q = 1; q < 13; q++) {
    issue(q + 3);
    wvb<12>();
  }
  const TW tw(p, lds, st, dt2, j);
  tw(0, 0);
  wvb<10>();
  tw(1, 1); tw(2, 2); tw(3, 7);
  wvb<16>();
  tw(4, 3); tw(5, 8);
  wvb<8>();
}
DI void g1_xloader(const Params& p, unsigned lds, int dt, int rot, int j, unsigned tag) {
  const int l = threadIdx.x & 63;
  rsrc_t rw = mk(p.ws, WS_BYTES);
  auto issue = [&](int n, int slot) {
    const unsigned so = WS_H + dt * (S * 128) + g1_stage(n, rot) * 16384 + j * 8192, xd = lds + G1_X + slot * G1_XS + j * 8192;
    dma16x4_sc1(rw, l * 16, so, xd);
    dma16x4_sc1(rw, l * 16, so + 4096, xd + 4096);
  };
  if (j == 0) {
    lds_wait(lds + L_CTL + 12, 1);
    __builtin_amdgcn_s_sleep(POLL_DELAY);
    poll_ln(rw, tag);
    lds_set(lds + L_CTL + 16);
  } else
    lds_wait(lds + L_CTL + 16, 1);
  issue(0, 0); issue(1, 1);
  wvb<8>();
  int slot = 2;
#pragma nounroll
  for (int n = 0; n < 14; n++) {
    issue(n + 2, slot);
    slot = slot == 2 ? 0 : slot + 1;
    wvb<8>();
  }
  wvb<0>();
}
template <int NP> DI void g1_loop(unsigned a0, unsigned b0, f32x16 (&acc)[2], bf16x8 (&fa)[4], bf16x8 (&fx)[4][2]) {
  unsigned aa = a0, ba = b0, bb = b0 ^ 64, xs = 0;
  auto rda = [&](int k, int ks) { fa[k] = trd2(aa + ks * 3072, aa + ks * 3072 + 768); fx[k][0] = trd2(ba + ks * 2048, ba + ks * 2048 + 512); };
  auto rdb = [&](int k, int ks) { fx[k][1] = trd2(bb + ks * 2048, bb + ks * 2048 + 512); };
  auto pair = [&](int par, int nx) {
#pragma unroll
    for (int u = 0; u < 2; u++) {
      const int k = 2 * par + u;
      acc[0] = mfma(fa[k], fx[k][0], acc[0]);
      SB0();
      rda(k ^ 2, 2 * nx + u);
      SB0();
      acc[1] = mfma(fa[k], fx[k][1], acc[1]);
      SB0();
      rdb(k ^ 2, 2 * nx + u);
      SB0();
    }
  };
  auto head = [&](int par) {
#pragma unroll
    for (int i = 0; i + 1 < NP; i++) pair((par + i) & 1, i + 1);
  };
  auto body = [&](int par, int n) {
    head(par);
    xs = xs == 2 * G1_XS ? 0 : xs + G1_XS;
    aa = a0 + (n & 3) * G1_WS; ba = b0 + xs; bb = ba ^ 64;
    bar();
    pair((par + NP - 1) & 1, 0);
  };
  rda(0, 0); rdb(0, 0); rda(1, 1); rdb(1, 1);
#pragma nounroll
  for (int n = 1; n < 15; n += 2) { body(0, n); body(NP & 1, n + 1); }
  body(0, 15);
  head(NP & 1);
  bar();
}
DI void g1_compute(const Params& p, unsigned lds, int dt, int ft, int c, TP& tp) {
  const int l = threadIdx.x & 63;
  rsrc_t rw = mk(p.ws, WS_BYTES), rb1 = mk(p.b1, F * 2);
  const int h = l >> 5, blk = (l >> 4) & 1, q = (l & 15) >> 2, pp = l & 3;
  const int nf = c % 3, kh = c / 3, k0 = kh * 4, pc = kh ? nf : nf + 3;
  const int fb = ft * 96 + nf * 32;
  const unsigned a0 = lds + G1_W + (k0 * 16 + 8 * h + q) * 192 + nf * 64 + blk * 32 + pp * 8;
  const unsigned b0 = (lds + G1_X + (k0 * 16 + 8 * h + q) * 128 + (((blk * 2 + (pp >> 1)) ^ (((q >> 1) & 1) << 2)) * 16) + (pp & 1) * 8) ^ (kh * 64);
  u32x2 bv[4];
#pragma unroll
  for (int g = 0; g < 4; g++) bv[g] = ld8(rb1, (8 * g + 4 * h + fb) * 2, 0);
  f32x16 acc[2] = {};
#pragma unroll
  for (int g = 0; g < 4; g++) { acc[0][4 * g] = blo(bv[g][0]); acc[0][4 * g + 1] = bhi(bv[g][0]); acc[0][4 * g + 2] = blo(bv[g][1]); acc[0][4 * g + 3] = bhi(bv[g][1]); }
  bf16x8 fa[4], fx[4][2];
  unsigned exw = lds + 3 * G1_WS + l * 16 + c * 4096, exr = lds + 3 * G1_WS + l * 16 + pc * 4096;
  unsigned so = fb < GT * 16 ? WS_G + ((dt * 2 + kh) * GT + ft * 6 + nf * 2) * 1024 + l * 16 : OOB;
  unsigned tm = p2_t0(lds, c) ^ ((c & 1) * 64);
  asm volatile("" : "+v"(tm));
  asm volatile("" : "+v"(exw), "+v"(exr), "+v"(so), "+v"(acc[0]), "+v"(acc[1]));
  bar();
  g1_loop<2>(a0, b0, acc, fa, fx);
  acc[1] = mfma(fa[2], fx[2][1], acc[1]);
  SB0();
  pull03(tp, tm, true);
  SB0();
  acc[1] = mfma(fa[3], fx[3][1], acc[1]);
  acc[0] = mfma(fa[2], fx[2][0], acc[0]);
  acc[0] = mfma(fa[3], fx[3][0], acc[0]);
  SB0();
#pragma unroll
  for (int g = 0; g < 4; g++) {
    f32x4 v = {acc[1][4 * g], acc[1][4 * g + 1], acc[1][4 * g + 2], acc[1][4 * g + 3]};
    *(LAS f32x4*)(exw + g * 1024) = v;
  }
  bar();
  u32x4 o[2];
#pragma unroll
  for (int g = 0; g < 4; g++) {
    const f32x4 v = *(LAS f32x4*)(exr + g * 1024);
#pragma unroll
    for (int jj = 0; jj < 2; jj++) {
      const f32x2 u = f32x2{acc[0][4 * g + 2 * jj], acc[0][4 * g + 2 * jj + 1]} + f32x2{v[2 * jj], v[2 * jj + 1]};
      const f32x2 z = u * (u * u * -0.1029432397f + -2.302208198f);
      const f32x2 d = f32x2{__builtin_amdgcn_exp2f(z[0]), __builtin_amdgcn_exp2f(z[1])} + 1.0f;
      const f32x2 r = u * f32x2{__builtin_amdgcn_rcpf(d[0]), __builtin_amdgcn_rcpf(d[1])};
      o[g >> 1][(g & 1) * 2 + jj] = pk(r[0], r[1]);
    }
  }
  st16<SC1>(o[0], rw, so);
  st16<SC1>(o[1], rw, so + 1024);
}

DI float max3(float a, float b, float c) { float r; asm("v_max3_f32 %0, %1, %2, %3" : "=v"(r) : "v"(a), "v"(b), "v"(c)); return r; }
DI void softmax16(f32x16 p, float sc, unsigned (&o)[8]) {
  asm volatile("s_nop 11" : "+v"(p));
  float m0 = max3(p[0], p[1], p[2]), m1 = max3(p[3], p[4], p[5]), m2 = max3(p[6], p[7], p[8]), m3 = max3(p[9], p[10], p[11]), m4 = max3(p[12], p[13], p[14]);
  const float nmx = -max3(max3(m0, m1, p[15]), max3(m2, m3, m4), m0) * sc;
  f32x2 e[8];
#pragma unroll
  for (int i = 0; i < 8; i++) {
    const f32x2 z = f32x2{p[2 * i], p[2 * i + 1]} * sc + nmx;
    e[i] = f32x2{__builtin_amdgcn_exp2f(z[0]), __builtin_amdgcn_exp2f(z[1])};
  }
  const f32x2 s2 = ((e[0] + e[1]) + (e[2] + e[3])) + ((e[4] + e[5]) + (e[6] + e[7]));
  const float s = __builtin_amdgcn_rcpf(s2[0] + s2[1]);
#pragma unroll
  for (int i = 0; i < 8; i++) { const f32x2 r = e[i] * s; o[i] = pk(r[0], r[1]); }
}
template <bool SC> DI void center(const f32x16& a, const f32x4 (&c)[4], float nm, float r, unsigned (&o)[8]) {
#pragma unroll
  for (int i = 0; i < 8; i++) {
    f32x2 v = f32x2{c[i >> 1][(i & 1) * 2], c[i >> 1][(i & 1) * 2 + 1]} * nm + f32x2{a[2 * i], a[2 * i + 1]};
    if (SC) v = v * r;
    o[i] = pk(v[0], v[1]);
  }
}
DI bf16x8 frag(const unsigned (&o)[8], int s) { u32x4 u = {o[4 * s], o[4 * s + 1], o[4 * s + 2], o[4 * s + 3]}; return __builtin_bit_cast(bf16x8, u); }

DI void g2_loader(const Params& p, unsigned lds, int rt, int hg, int rot, int j) {
  const int l = threadIdx.x & 63;
  rsrc_t rx = mk(p.x, S * D * 2), rq = mk(p.wqkv, D * 3 * D * 2), rp = mk(p.wphi, 32 * 256 * 2);
  unsigned xv[2];
#pragma unroll
  for (int i = 0; i < 2; i++) xv[i] = (j * 32 + i * 8 + (l >> 3)) * 1024 + (((l & 7) ^ ((4 * i + (l >> 4)) & 7)) * 16);
  const unsigned wv = (j * 16 + (l >> 3)) * 3072 + (((l & 7) ^ (((l >> 4) & 1) << 2)) * 16);
  auto issue = [&](int s, int slot) {
    const unsigned sidx = (s + rot) & 7, base = lds + slot * G2_SLOT;
    const unsigned xo = rt * (128 * 1024) + sidx * 128, wo = sidx * (64 * 3072) + hg * 128;
#pragma unroll
    for (int i = 0; i < 2; i++) {
      dma16(rx, xv[0], xo + i * 16384, base + G2_XO + j * 4096 + i * 2048);
      dma16(rx, xv[1], xo + i * 16384, base + G2_XO + j * 4096 + i * 2048 + 1024);
#pragma unroll
      for (int c = 0; c < 3; c++) dma16(rq, wv, wo + i * (8 * 3072) + c * 1024, base + G2_WO + c * 8192 + j * 2048 + i * 1024);
    }
  };
  issue(0, 0);
  issue(1, 1);
  wvb<10>();
  int slot = 2;
  for (int s = 0; s < 6; s++) {
    issue(s + 2, slot);
    slot = slot == 2 ? 0 : slot + 1;
    wvb<10>();
  }
  if (j < 2) dma16(rp, l * 16 + j * 1024, hg * 2048, lds + G2_PHI + j * 1024);
  {
    rsrc_t rw = mk(p.ws, WS_BYTES);
    const unsigned tag = lds_tag(lds);
    const unsigned off = j < 2 ? WS_SB + (rt * 128 + j * 64 + l) * 16 : WS_CS + hg * 1536 + (j - 2) * 768 + (l < 48 ? l : 0) * 16;
    u32x4 g;
#pragma nounroll
    for (int sp = 0; sp < SPIN_CAP; sp++) {
      asm volatile("" ::: "memory");
      g = ld16<SC1>(rw, off, 0);
      if (__all(g[1] == tag && g[3] == ~tag)) break;
    }
    u32x2 o = {g[0], g[2]};
    if (j < 2 || l < 48) *(LAS u32x2*)(lds + (j < 2 ? G2_ST + j * 512 : G2_CS + (j - 2) * 384) + l * 8) = o;
  }
  wvb<0>();
}
DI void cs_phase(const Params& p, unsigned lds, int cg, int j) {
  const int l = threadIdx.x & 63;
  rsrc_t rq = mk(p.wqkv, D * 3 * D * 2), rw = mk(p.ws, WS_BYTES);
  float s[8] = {};
  unsigned sel_lo = 0x00003f80u;
  asm("" : "+s"(sel_lo));
  const unsigned vo = (j * 128 + (l >> 3)) * 3072 + (l & 7) * 16 + cg * 128;
  u32x4 v[16];
#pragma unroll
  for (int u = 0; u < 16; u++) v[u] = ld16(rq, vo, u * (8 * 3072));
  SB0();
#pragma unroll
  for (int u = 0; u < 16; u++)
#pragma unroll
    for (int jj = 0; jj < 4; jj++) { s[2 * jj] = dot2(v[u][jj], sel_lo, s[2 * jj]); s[2 * jj + 1] = dot2(v[u][jj], 0x3f800000u, s[2 * jj + 1]); }
  const unsigned sc = lds + L_MISC;
  const unsigned pa = sc + (j * 8 + (l >> 3)) * 272 + (l & 7) * 32;
  f32x4 a = {s[0], s[1], s[2], s[3]}, b = {s[4], s[5], s[6], s[7]};
  *(LAS f32x4*)pa = a;
  *(LAS f32x4*)(pa + 16) = b;
  if (lds_arrive(lds + L_CTL + 28, 4)) {
    float t = 0.f;
#pragma unroll
    for (int r = 0; r < 32; r++) t += *(LAS float*)(sc + r * 272 + l * 4);
    lds_wait(lds + L_CTL + 52, 1);
    const unsigned tag = lds_tag(lds);
    u32x2 g = {asu(t), (l & 1) ? ~tag : tag};
    __builtin_amdgcn_raw_buffer_store_b64(g, rw, WS_CS + (cg & 7) * 1536 + (((l >> 5) * 3 + (cg >> 3)) * 32 + (l & 31)) * 8, 0, SC1);
  }
}

DI void g2_kv(const Params& p, unsigned lds, int rt, int hg, int w) {
  const int l = threadIdx.x & 63;
  rsrc_t rw = mk(p.ws, WS_BYTES);
  const int h = l >> 5, blk = (l >> 4) & 1, q = (l & 15) >> 2, pp = l & 3;
  const unsigned pwa = lds + L_PW + (l & 7) * 4;
    unsigned aa[2], ba[2], bs = blk ? 0 : 2048;
#pragma unroll
    for (int tt = 0; tt < 2; tt++) {
      unsigned sr = 8 * h + 4 * tt + q;
      unsigned f1 = ((sr & 3) << 3) | ((sr >> 1) & 7), f2 = ((sr << 1) | ((sr >> 3) & 1)) & 15;
      aa[tt] = lds + G2_PK + sr * 256 + (((w * 8 + blk * 4 + pp) ^ f1) * 8);
      ba[tt] = blk ? lds + G2_ONES + pp * 8 : lds + G2_V + sr * 128 + (((w * 4 + pp) ^ f2) * 8);
    }
    unsigned bb[8][2];
#pragma unroll
    for (int ks = 0; ks < 8; ks++) { bb[ks][0] = ba[0] + ks * bs; bb[ks][1] = ba[1] + ks * bs; asm volatile("" : "+v"(bb[ks][0]), "+v"(bb[ks][1])); }
    const int head = hg * 4 + w, c = l & 31;
    unsigned off = c < 16 ? WS_KVP + (rt * 32 + head) * 1024 + (h * 16 + c) * 16 : c == 16 ? WS_ZP + head * 1024 + (h * 16 + rt) * 16 : OOB;
    asm volatile("" : "+v"(off), "+v"(aa[0]), "+v"(aa[1]));
    bar();
    __builtin_amdgcn_s_sleep(16);
    __builtin_amdgcn_s_setprio(2);
    prog_wait(pwa, 1);
    bf16x8 ka[8], kb[8];
#pragma unroll
    for (int ks = 0; ks < 8; ks++) {
      ka[ks] = trd2(aa[0] + ks * 4096, aa[1] + ks * 4096);
      kb[ks] = trd2(bb[ks][0], bb[ks][1]);
    }
    SB0();
    f32x16 kv = {};
#pragma unroll
    for (int ks = 0; ks < 8; ks++) kv = mfma(ka[ks], kb[ks], kv);

    st16<SC1>(pack8u(kv, 0), rw, off);
    st16<SC1>(pack8u(kv, 1), rw, off + 512);
    drain();
    if (lds_arrive(lds + L_CTL, 4)) {
      if (l == 0) put_flag(rw, WS_F2 + (hg * 16 + rt) * 128, lds_tag(lds));
    }
    __builtin_amdgcn_s_setprio(0);
}

DI void g2_compute(const Params& p, unsigned lds, int rt, int hg, int w) {
  const int t = threadIdx.x, l = t & 63;
  rsrc_t rw = mk(p.ws, WS_BYTES);
  if (t < 16) *(LAS unsigned*)(lds + G2_ONES + t * 4) = 0x3f803f80u;
  const int h = l >> 5, blk = (l >> 4) & 1, q = (l & 15) >> 2, pp = l & 3;
  const int mq = w >> 1, hp = w & 1;
  const unsigned xrow = mq * 32 + (l & 31);
  unsigned xa[4];
#pragma unroll
  for (int k = 0; k < 4; k++) xa[k] = (lds + G2_XO + xrow * 128 + ((h ^ ((xrow >> 1) & 7)) * 16)) ^ (k << 5);
  const unsigned wa_ = lds + G2_WO + (8 * h + q) * 128 + (((hp * 4 + blk * 2 + (pp >> 1)) ^ (((q >> 1) & 1) << 2)) * 16) + (pp & 1) * 8;
  f32x16 acc[3] = {};
  bf16x8 fx[4], fw[4][3];
  unsigned sb = 0;
  auto rd = [&](int k, int i) {
    if (i == 0) fx[k] = lds16(xa[k] + sb);
    const unsigned wb = wa_ + sb + k * 2048 + i * 8192;
    fw[k][i] = trd2(wb, wb + 512);
  };
  auto step = [&](int k, bool more) {
#pragma unroll
    for (int i = 0; i < 3; i++) {
      acc[i] = mfma(fw[k][i], fx[k], acc[i]);
      SB0();
      if (more && k >= 2) rd(k ^ 2, i);
      if (more && k == 0) { rd(2, i); if (i == 2) rd(3, 0); }
      if (more && k == 1 && i == 0) { rd(3, 1); rd(3, 2); }
      SB0();
    }
  };
  unsigned pka = (lds + G2_PK + xrow * 256 + (((xrow & 3) << 3) | ((xrow >> 1) & 7)) * 8) ^ ((hp * 16 + h) * 8);
  unsigned va = (lds + G2_V + xrow * 128 + (((xrow << 1) | ((xrow >> 3) & 1)) & 15) * 8) ^ ((hp * 8 + h) * 8);
  unsigned csa = lds + G2_CS + (hp * 96 + 4 * h) * 4;
  asm volatile("" : "+v"(acc[0]), "+v"(acc[1]), "+v"(acc[2]), "+v"(pka), "+v"(va), "+v"(csa));
  bar();
#pragma unroll
  for (int i = 0; i < 6; i++) rd(i / 3, i % 3);
#pragma nounroll
  for (int s = 1; s < 8; s++) {
    step(0, true);
    step(1, true);
    sb = sb == 2 * G2_SLOT ? 0 : sb + G2_SLOT;
    bar();
    step(2, true);
    step(3, true);
  }
  step(0, true);
  step(1, true);
  bar();
  unsigned tch = 0;
  {
    const int u = (rt & 3) * 8 + hg, row = w * 8 + (l >> 3);
    rsrc_t rt_ = w < 6 ? mk(p.w2, F * S * 2) : mk(p.wo, D * D * 2);
    const unsigned o = w < 6 ? (row < 43 ? (u * 43 + row) * 4096 + (rt >> 2) * 1024 + (l & 7) * 128 : OOB) : (w == 6 && l < 32 ? ((rt * 8 + hg) * 32 + l) * 128 : OOB);
    asm volatile("buffer_load_dword %0, %1, %2, 0 offen" : "=v"(tch) : "v"(o), "s"(rt_) : "memory");
  }
  __builtin_amdgcn_s_setprio(1);
  step(2, false);
  f32x4 ck[4], cv[4];
#pragma unroll
  for (int i = 0; i < 4; i++) { ck[i] = *(LAS f32x4*)(csa + 128 + 32 * i); cv[i] = *(LAS f32x4*)(csa + 256 + 32 * i); }
  const f32x2 rm = *(LAS f32x2*)(lds + G2_ST + xrow * 8);
  bf16x8 wf[2];
#pragma unroll
  for (int e = 0; e < 2; e++) {
    unsigned a = lds + G2_PHI + (hp * 2 + e) * 512 + (4 * h + q) * 32 + (2 * blk + (pp >> 1)) * 8;
    wf[e] = trd2(a, a + 8 * 32);
  }
  SB0();
  acc[1] = mfma(fw[3][1], fx[3], acc[1]);
  acc[2] = mfma(fw[3][2], fx[3], acc[2]);
  acc[0] = mfma(fw[3][0], fx[3], acc[0]);
  SB0();
#pragma unroll
  for (int e = 0; e < 2; e++) {
    u32x4 u = __builtin_bit_cast(u32x4, wf[e]);
    unsigned sgn = (q & 1) ? 0x80008000u : 0u;
#pragma unroll
    for (int jj = 0; jj < 4; jj++) u[jj] ^= sgn;
    wf[e] = __builtin_bit_cast(bf16x8, u);
  }
  const float r = rm[0], nm = -rm[1], sc = r * 1.4426950408889634f;
  const f32x16 z16 = {};
  {
    unsigned o[8];
    center<false>(acc[1], ck, nm, r, o);
    f32x16 pk0 = mfma(wf[0], frag(o, 0), z16), pk1 = mfma(wf[1], frag(o, 1), z16);
    SB0();
    center<true>(acc[2], cv, nm, r, o);
#pragma unroll
    for (int g = 0; g < 4; g++) *(LAS u32x2*)(va ^ (g * 16)) = u32x2{o[2 * g], o[2 * g + 1]};
    softmax16(pk0, sc, o);
#pragma unroll
    for (int g = 0; g < 4; g++) *(LAS u32x2*)(pka ^ (g * 16)) = u32x2{o[2 * g], o[2 * g + 1]};
    softmax16(pk1, sc, o);
#pragma unroll
    for (int g = 0; g < 4; g++) *(LAS u32x2*)(pka ^ (64 + g * 16)) = u32x2{o[2 * g], o[2 * g + 1]};
  }
  const unsigned pwm = lds + L_PW + w * 4, pwa = lds + L_PW + (l & 7) * 4;
  prog_put(pwm, 1);
  SB0();
  __builtin_amdgcn_s_setprio(0);
  bf16x8 phq[2][2];
  {
    f32x4 cq[4];
#pragma unroll
    for (int i = 0; i < 4; i++) cq[i] = *(LAS f32x4*)(csa + 32 * i);
    unsigned o[8];
    center<false>(acc[0], cq, nm, r, o);
    f32x16 pq0 = mfma(wf[0], frag(o, 0), z16), pq1 = mfma(wf[1], frag(o, 1), z16);
    softmax16(pq0, sc, o);
    phq[0][0] = frag(o, 0); phq[0][1] = frag(o, 1);
    softmax16(pq1, sc, o);
    phq[1][0] = frag(o, 0); phq[1][1] = frag(o, 1);
  }
  const int hh = w & 3, ee = w >> 2, head = hg * 4 + hh;
  unsigned ko = WS_KVP + ((l >> 5) * 32 + head) * 1024 + ee * 512 + (l & 31) * 16, zo = l < 32 ? OOB : WS_ZP + head * 1024 + ee * 512 + (l & 31) * 16;
  unsigned ta = lds + G2_T + hh * 2048 + ee * 1024 + (((l >> 4) & 1) * 32 + (l & 15)) * 16 + (l >> 5) * 8;
  u32x4 sel;
#pragma unroll
  for (int i = 0; i < 4; i++) sel[i] = ((l & 31) >> 1) == i ? ((l & 1) ? 0x3f800000u : 0x00003f80u) : 0u;
  unsigned tr_ = lds + G2_T + hp * 4096 + l * 16, oo = WS_O + l * 16 + ((rt * 4 + mq) * 32 + hg * 4 + hp * 2) * 1024;
  asm volatile("" : "+v"(sel), "+v"(ko), "+v"(zo), "+v"(ta), "+v"(tr_), "+v"(oo), "+v"(phq[0][0]), "+v"(phq[0][1]), "+v"(phq[1][0]), "+v"(phq[1][1]));
  if (w == 7) {
    lds_wait(lds + L_CTL, 4);
    __builtin_amdgcn_s_sleep(POLL_DELAY);
    poll_flags(rw, WS_F2 + (hg * 16 + (l & 15)) * 128, lds_tag(lds));
    lds_set(lds + L_CTL + 12);
  }
  lds_wait(lds + L_CTL + 12, 1);
  {
    u32x4 v[8];
    u32x4 zv = ld16<SC1>(rw, zo, 0);
#pragma unroll
    for (int i = 0; i < 8; i++) v[i] = ld16<SC1>(rw, ko, i * 65536);
    SB0();
    if (l >= 32) *(LAS u32x4*)(ta + 248) = zv;
    f32x16 d = {};
#pragma unroll
    for (int i = 0; i < 8; i++) d = mfma(__builtin_bit_cast(bf16x8, sel), __builtin_bit_cast(bf16x8, v[i]), d);
    u32x2 o = {pk(d[0], d[1]), pk(d[2], d[3])};
    asm volatile("" : "+v"(o) : "v"(tch));
    *(LAS u32x2*)ta = o;
  }
  prog_put(pwm, 2);
  prog_wait(pwa, 2);
  bf16x8 ta_[2][2];
#pragma unroll
  for (int e = 0; e < 2; e++)
#pragma unroll
    for (int c = 0; c < 2; c++) ta_[e][c] = lds16(tr_ + e * 2048 + c * 1024);
  SB0();
  f32x16 a[2];
#pragma unroll
  for (int e = 0; e < 2; e++) a[e] = mfma(ta_[e][1], phq[e][1], mfma(ta_[e][0], phq[e][0], z16));
#pragma unroll
  for (int e = 0; e < 2; e++) {
    const f32x2 d2 = (f32x2{a[e][8], a[e][9]} + f32x2{a[e][10], a[e][11]}) + (f32x2{a[e][12], a[e][13]} + f32x2{a[e][14], a[e][15]});
    const float den = __builtin_amdgcn_rcpf(half_sum(d2[0] + d2[1]) + 1e-6f);
    u32x4 o;
#pragma unroll
    for (int i = 0; i < 4; i++) { const f32x2 u = f32x2{a[e][2 * i], a[e][2 * i + 1]} * den; o[i] = pk(u[0], u[1]); }
    st16<SC1>(o, rw, oo + e * 1024);
  }
}

DI void p2_loader(const Params& p, unsigned lds, int st, int dt, int j, bool g1t) {
  const int l = threadIdx.x & 63;
  rsrc_t rwo = mk(p.wo, D * D * 2), rw = mk(p.ws, WS_BYTES);
  const unsigned tvo = (j * 32 + (l >> 3)) * 1024 + ((l & 7) ^ (((l >> 4) & 1) << 2)) * 16, base = lds + j * 4096;
  const TW tw(p, lds, st, dt, j);
  const int rot = tw.rot;
  bar();
  tw(6, 6); tw(7, 7); tw(8, 0); tw(9, 1); tw(10, 2);
  wvb<0>();
  bar();
  auto two = [&](int u) {
    const unsigned to = (((u >> 1) + rot) & 3) * (128 * 1024) + dt * 128 + (u & 1) * (16 * 1024), dst = base + (u >> 1) * P2_S + (u & 1) * 2048;
    dma16(rwo, tvo, to, dst);
    dma16(rwo, tvo, to + 8 * 1024, dst + 1024);
  };
#pragma nounroll
  for (int u = 0; u < 8; u++) two(u);
  const unsigned g0 = WS_G + (dt * 2 + (j >> 1)) * GT * 1024 + (j & 1) * 4096, o0 = WS_O + (st * 2 + (j >> 1)) * 32768 + (j & 1) * 4096;
  unsigned gc = (rot % 11) * 8192, vl = l * 16;
  const unsigned f3 = WS_F3 + (dt * 16 + ((l & 15) < 15 ? (l & 15) : 0)) * 128, tag = lds_tag(lds);
  auto issue = [&](int s) {
    if (s >= 4) { dma16x4_sc1(rw, vl, g0 + gc, base + (4 + s % 5) * P2_S); gc = gc == 10 * 8192 ? 0 : gc + 8192; }
    else dma16x4_sc1(rw, vl, o0 + ((s + rot) & 3) * 8192, base + (4 + s % 5) * P2_S);
  };
  bar();
  u32x2 fg;
  asm volatile("buffer_load_dwordx2 %0, %1, %2, 0 offen sc1" : "=v"(fg) : "v"(f3), "s"(rw) : "memory");
#pragma unroll
  for (int s = 0; s < 4; s++) issue(s);
  wvb<12>();
  asm volatile("" : "+v"(fg));
  if (__all(fg[0] == tag && fg[1] == ~tag)) {
#pragma unroll
    for (int s = 0; s < 4; s++) { issue(s + 4); wvb<12>(); }
  } else {
    wvb<8>(); wvb<4>(); wvb<0>();
    poll_flags(rw, f3, tag);
#pragma unroll
    for (int s = 4; s < 8; s++) issue(s);
    wvb<12>();
  }
#pragma unroll
  for (int s = 4; s < 11; s++) { issue(s + 4); wvb<12>(); }
  wvb<8>(); wvb<4>(); wvb<0>();
  bar();
  bar();
}
DI void p2_compute(const Params& p, unsigned lds, int st, int dt, int w, unsigned pflag, bool g1t, TP& tp) {
  const int t = threadIdx.x, l = t & 63;
  rsrc_t rx = mk(p.x, S * D * 2), rb2 = mk(p.b2, S * 2), ro = mk(p.out, S * D * 2);
  const unsigned ov = (t >> 3) * 1024 + (t & 7) * 16 + st * 65536 + dt * 128;
  const int h = l >> 5, blk = (l >> 4) & 1, q = (l & 15) >> 2, pp = l & 3;
  const int kq = w >> 1, ms = w & 1;
  const unsigned t0 = p2_t0(lds, w), tm = t0 ^ (ms * 64), t1 = t0 ^ 64;
  rsrc_t rw = mk(p.ws, WS_BYTES);
  drain();
  if (pflag) {
    if (lds_arrive(lds + L_CTL + 56, g1t ? 6 : 8)) {
      if (l == 0) put_flag(rw, pflag, lds_tag(lds));
    }
  }
  u32x4 xres;
  unsigned short b2v;
  bar();
  pull(tp, 4, tm + 3 * P2_S); pull(tp, 8, tm); pull(tp, 9, tm + P2_S); pull(tp, 10, tm + 2 * P2_S);
  bar();
  pull(tp, 5, tm + (g1t ? 8 : 5) * P2_S); pull(tp, 6, tm + 6 * P2_S); pull(tp, 7, tm + 7 * P2_S);
  f32x16 acc[2] = {};
  asm volatile("" : "+v"(acc[0]), "+v"(acc[1]));
  if (w == 7) poll_flags(rw, WS_F4 + ((st >> 1) * 8 + (l & 7)) * 128, lds_tag(lds));
  bar();
  const unsigned la = lds + 4 * P2_S + kq * 2048 + l * 16, la4 = la + 4 * P2_S;
  bf16x8 fa[2][2][2], fo[2][2];
  auto rd = [&](int s, int k) {
    const int buf = s & 1, sl = s % 5;
    const unsigned a = (sl < 4 ? la + sl * P2_S : la4) + k * 1024;
    if (s >= 4) {
      fa[buf][k][0] = lds16(a);
      fa[buf][k][1] = lds16(a + 8192);
    } else {
      fo[buf][k] = lds16(a + ms * 8192);
      const unsigned o = s * P2_S + k * 2048;
      fa[buf][k][0] = trd2(t0 + o, t0 + o + 1024);
      fa[buf][k][1] = trd2(t1 + o, t1 + o + 1024);
    }
  };
  bar();
  rd(0, 0); rd(0, 1);
#pragma unroll
  for (int s = 0; s < 15; s++) {
    bar();
#pragma unroll
    for (int k = 0; k < 2; k++) {
      const bf16x8 bop = s >= 4 ? tp.f[s >= 4 ? s - 4 : 0][k] : fo[s & 1][k];
      acc[0] = mfma(fa[s & 1][k][0], bop, acc[0]);
      SB0();
      if (s >= 4 && s < 14 && k == 0) { rd(s + 1, 0); rd(s + 1, 1); }
      if (s < 4 && k == 1) rd(s + 1, 1);
      SB0();
      acc[1] = mfma(fa[s & 1][k][1], bop, acc[1]);
      SB0();
      if (s < 4 && k == 0) rd(s + 1, 0);
      if (s == 7 && k == 0) {
        xres = ld16(rx, ov, 0);
        b2v = __builtin_amdgcn_raw_buffer_load_b16(rb2, (t >> 3) * 2, st * 128, 0);
      }
      SB0();
    }
  }
#pragma unroll
  for (int nd = 0; nd < 2; nd++)
#pragma unroll
    for (int g = 0; g < 4; g++) {
      f32x4 v = {acc[nd][4 * g], acc[nd][4 * g + 1], acc[nd][4 * g + 2], acc[nd][4 * g + 3]};
      *(LAS f32x4*)(lds + kq * 17408 + (ms * 32 + (l & 31)) * 272 + (nd * 32 + 8 * g + 4 * h) * 4) = v;
    }
  bar();
  {
    const unsigned ra = lds + (t >> 3) * 272 + (t & 7) * 32;
    f32x4 a0 = (*(LAS f32x4*)ra + *(LAS f32x4*)(ra + 17408)) + (*(LAS f32x4*)(ra + 2 * 17408) + *(LAS f32x4*)(ra + 3 * 17408));
    f32x4 a1 = (*(LAS f32x4*)(ra + 16) + *(LAS f32x4*)(ra + 17408 + 16)) + (*(LAS f32x4*)(ra + 2 * 17408 + 16) + *(LAS f32x4*)(ra + 3 * 17408 + 16));
    const float bb = blo(b2v);
    u32x4 o = {pk(a0[0] + bb + blo(xres[0]), a0[1] + bb + bhi(xres[0])), pk(a0[2] + bb + blo(xres[1]), a0[3] + bb + bhi(xres[1])),
               pk(a1[0] + bb + blo(xres[2]), a1[1] + bb + bhi(xres[2])), pk(a1[2] + bb + blo(xres[3]), a1[3] + bb + bhi(xres[3]))};
    st16(o, ro, ov);
  }
}

extern __shared__ __attribute__((aligned(128))) char smem[];
DI unsigned lds_base() { return (unsigned)(size_t)(LAS char*)smem; }

__global__ __launch_bounds__(NT, 3) void k_main(Params p) {
  const int b = blockIdx.x, xr = (b + 7) & 7, i = b >> 3, l = threadIdx.x & 63, w = __builtin_amdgcn_readfirstlane(threadIdx.x >> 6);
  const unsigned lds = lds_base();
  if ((w == 6 || w == 7) && i == 0) {
    unsigned long long pc0;
    unsigned sink;
    asm volatile("s_getpc_b64 %0" : "=s"(pc0));
    rsrc_t rc = mk((const void*)(pc0 & ~127ull), CODE_BYTES);
    asm volatile("buffer_load_dword %0, %1, %2, 0 offen\n\tbuffer_load_dword %0, %1, %2, %3 offen\n\ts_waitcnt vmcnt(0)"
                 : "=&v"(sink) : "v"(((w - 6) * 64 + l) * 128), "s"(rc), "s"(16384) : "memory");
  }
  rsrc_t rw = mk(p.ws, WS_BYTES);
  const int st = (xr >> 1) * 8 + (i & 7), dt2 = (xr & 1) * 4 + (i >> 3);
  const bool g1 = i < 16;
  const bool crew = g1 && (w < 6 || w >= 10);
  const int c = w < 6 ? w : w - 4, lb = xr * 16 + (i & 15);
  u32x4 v0, v1;
  if (crew) {
    __builtin_amdgcn_s_setprio(3);
    rsrc_t rx = mk(p.x, S * D * 2);
    const unsigned o = (lb * 16 + c * 2 + (l >> 5)) * 1024 + (l & 31) * 16;
    v0 = ld16(rx, o, 0);
    v1 = ld16(rx, o + 512, 0);
  }
  unsigned seedv = 0;
  if (crew || w == 0) seedv = __builtin_amdgcn_raw_buffer_load_b32(rw, WS_SEED, 0, SC1);
  if (threadIdx.x < 128) *(LAS unsigned*)(lds + L_CTL + threadIdx.x * 4) = 0u;
  bar();
  if (w == 0) { *(volatile LAS unsigned*)(lds + L_CTL + 24) = next_tag(seedv); lds_set(lds + L_CTL + 52); }
  unsigned pflag = 0;
  bool g1t = false, pulled = false;
  TP tp;
  if (g1) {
    const int dt = (xr & 1) * 4 + (i & 3), ft = (xr >> 1) * 4 + (i >> 2);
    const int rot = ((2 * ((i & 3) + (i >> 2)) + (xr >> 1)) & 7) | ((xr & 1) << 3);
    if (!crew) {
      if (ft >= 13) cs_phase(p, lds, (ft - 13) * 8 + dt, w - 6);
      if (ft < 15) g1_wloader(p, lds, ft, rot, w - 6, st, dt2);
    } else {
      const unsigned tag = next_tag(__builtin_amdgcn_readfirstlane(seedv));
      ln_phase(p, lds, lb, c, tag, v0, v1);
      if (ft < 15) {
        if (w < 6) {
          __builtin_amdgcn_s_setprio(0);
          g1_compute(p, lds, dt, ft, w, tp);
          pulled = true;
          pflag = WS_F3 + (dt * 16 + ft) * 128;
        } else {
          g1_xloader(p, lds, dt, rot, w - 10, tag);
          bar();
        }
      } else if (w == 0) {
        __builtin_amdgcn_s_sleep(IDLE_SLP);
        poll_ln<IDLE_SLP>(rw, tag);
        poll_flags<IDLE_SLP>(rw, WS_F2 + l * 128, tag);
        poll_flags<IDLE_SLP>(rw, WS_F2 + 8192 + l * 128, tag);
        if (dt == 0 && l == 0) __builtin_amdgcn_raw_buffer_store_b32(tag, rw, WS_SEED, 0, SC1);
      }
    }
    g1t = ft < 15;
    if (!g1t) {
      if (w >= 8) {
        const TW tw(p, lds, st, dt2, w - 8);
#pragma nounroll
        for (int s = 0; s < 6; s++) tw(s, s == 3 ? 4 : s == 4 ? 3 : s);
      }
      wvb<0>();
    }
  } else {
    const int rt = (xr >> 1) * 4 + (i & 3), hg = (xr & 1) * 4 + ((i >> 2) & 3), rot = ((i & 3) * 2 + ((i >> 2) & 3) * 3) & 7;
    if (w >= 8) {
      g2_loader(p, lds, rt, hg, rot, w - 8);
      g2_kv(p, lds, rt, hg, w - 8);
      const TW tw(p, lds, st, dt2, w - 8);
#pragma nounroll
      for (int s = 0; s < 6; s++) tw(s, s == 3 ? 4 : s == 4 ? 3 : s);
      wvb<0>();
    } else {
      g2_compute(p, lds, rt, hg, w);
      pflag = WS_F4 + (rt * 8 + hg) * 128;
      bar();
    }
  }
  if (w < 8) {
    if (!pulled) {
      pull03(tp, p2_t0(lds, w) ^ ((w & 1) * 64), g1t);
      bar();
    }
    p2_compute(p, lds, st, dt2, w, pflag, g1t, tp);
  } else
    p2_loader(p, lds, st, dt2, w - 8, g1t);
}
}

extern "C" void kernel_launch(void* const* d_in, const int* in_sizes, int n_in, void* d_out, int out_size, void* d_ws, size_t ws_size,
                              hipStream_t stream) {
  using namespace hk;
  Params p{d_in[0], d_in[1], d_in[2], d_in[3], d_in[5], d_in[6], d_in[7], d_in[8], d_out, d_ws};
  (void)hipFuncSetAttribute((const void*)k_main, hipFuncAttributeMaxDynamicSharedMemorySize, LDS_BYTES);
  k_main<<<256, NT, LDS_BYTES, stream>>>(p);
}
```
